```python
import torch

def _softmax(x, axis=-1):
    return torch.softmax(x, dim=axis)
def _gelu(x):
    return torch.nn.functional.gelu(x, approximate="tanh")
def _silu(x):
    return torch.nn.functional.silu(x)
def _sigmoid(x):
    return torch.sigmoid(x)
def _layernorm(x, axis=-1, eps=1e-5):
    m = x.mean(dim=axis, keepdim=True)
    v = x.var(dim=axis, keepdim=True, unbiased=False)
    return (x - m) / torch.sqrt(v + eps)
def _scatter_add(tgt, idx, src, axis=0):
    return tgt.clone().index_add_(axis, idx, src)
def _segment_sum(src, seg, n):
    out = torch.zeros((n,) + src.shape[1:], dtype=src.dtype)
    return out.index_add_(0, seg, src)
def _pad(x, pw):
    flat = tuple(v for pair in reversed(pw) for v in pair)
    return torch.nn.functional.pad(x, flat)

kernel_name = 'concat_heads__softmax_full_mha__spectral_fnet__prompt'

def setup_inputs():

    _w = (lambda a: (lambda t: t.to(torch.bfloat16) if t.is_floating_point() else t)(torch.as_tensor(a)))
    def _A_setup():
        B, S, D = 1, 2048, 448
        return {
            "x": _w((lambda *s: torch.randn(*s, dtype=torch.bfloat16) * 0.2)(B, S, D)),
            "wqkv": _w((lambda *s: torch.randn(*s, dtype=torch.bfloat16) * 0.8)(D, 3 * D)),
            "wo": _w((lambda *s: torch.randn(*s, dtype=torch.bfloat16) * 0.2)(D, D)),
        }
    _A_in = _A_setup()

    def _B_setup():
        B, S, D = 1, 2048, 448
        ns = torch.arange(S)[:, None] * torch.arange(S)[None, :]
        nd = torch.arange(D)[:, None] * torch.arange(D)[None, :]
        Ws_re = torch.cos(-2.0 * torch.pi * ns / S).to(float)
        Ws_im = torch.sin(-2.0 * torch.pi * ns / S).to(float)
        Wd_re = torch.cos(-2.0 * torch.pi * nd / D).to(float)
        Wd_im = torch.sin(-2.0 * torch.pi * nd / D).to(float)
        return {
            "x": _w((lambda *s: torch.randn(*s, dtype=torch.bfloat16) * 0.05)(B, S, D)),
            "dft_seq_re": _w(Ws_re),
            "dft_seq_im": _w(Ws_im),
            "dft_hid_re": _w(Wd_re),
            "dft_hid_im": _w(Wd_im),
        }
    _B_in = _B_setup()

    return {
        "A_x": _A_in["x"],
        "A_wqkv": _A_in["wqkv"],
        "A_wo": _A_in["wo"],
        "B_x": _B_in["x"],
        "B_dft_seq_re": _B_in["dft_seq_re"],
        "B_dft_seq_im": _B_in["dft_seq_im"],
        "B_dft_hid_re": _B_in["dft_hid_re"],
        "B_dft_hid_im": _B_in["dft_hid_im"],
    }

def reference(A_x, A_wqkv, A_wo, B_x, B_dft_seq_re, B_dft_seq_im, B_dft_hid_re, B_dft_hid_im):
    def _A_ref(x, wqkv, wo):
        B, S, D = x.shape
        H = 16; Dh = D // H
        qkv = (lambda x, *d: x.permute(*d))((x @ wqkv).reshape(B, S, 3, H, Dh), 2, 0, 3, 1, 4)
        q, k, v = qkv[0], qkv[1], qkv[2]
        scale = Dh ** -0.5
        scores = torch.einsum("bhqd,bhkd->bhqk", q, k) * scale
        p = _softmax(scores, axis=-1)
        o = torch.einsum("bhqk,bhkd->bhqd", p, v)
        return (lambda x, *d: x.permute(*d))(o, 0, 2, 1, 3).reshape(B, S, D) @ wo

    def _B_ref(x, dft_seq_re, dft_seq_im, dft_hid_re, dft_hid_im):
        a = torch.einsum("bsd,ed->bse", x, dft_hid_re)
        b = torch.einsum("bsd,ed->bse", x, dft_hid_im)
        y = torch.einsum("ts,bse->bte", dft_seq_re, a) - torch.einsum("ts,bse->bte", dft_seq_im, b)
        return y

    a = _A_ref(A_x, A_wqkv, A_wo)
    b = _B_ref(B_x, B_dft_seq_re, B_dft_seq_im, B_dft_hid_re, B_dft_hid_im)
    return torch.concatenate([a, b], axis=-1)
```

```cpp
#include <hip/hip_runtime.h>
#include <hip/hip_bf16.h>
#include <stdint.h>
#include <utility>

typedef __bf16 bf16x8 __attribute__((ext_vector_type(8)));
typedef __bf16 bf16x4 __attribute__((ext_vector_type(4)));
typedef __bf16 bf16x2 __attribute__((ext_vector_type(2)));
typedef short s16x4 __attribute__((ext_vector_type(4)));
typedef float f32x4 __attribute__((ext_vector_type(4)));
typedef float f32x16 __attribute__((ext_vector_type(16)));
typedef unsigned u32x4 __attribute__((ext_vector_type(4)));
typedef unsigned u32x2 __attribute__((ext_vector_type(2)));
#define LDSP __attribute__((address_space(3)))

constexpr int SEQ = 2048, DM = 448, NH = 16, DH = 28;

constexpr size_t WS_Q = 0;
constexpr size_t WS_K = 2u << 20;
constexpr size_t WS_V = 4u << 20;
constexpr size_t WS_B = 6u << 20;
constexpr size_t WS_O = 10u << 20;
constexpr size_t WS_P = 12u << 20;

template <int... Is, class F>
__device__ __forceinline__ void static_for_impl(std::integer_sequence<int, Is...>, F&& f) {
  (f(std::integral_constant<int, Is>{}), ...);
}
template <int N, class F>
__device__ __forceinline__ void static_for(F&& f) {
  static_for_impl(std::make_integer_sequence<int, N>{}, static_cast<F&&>(f));
}

__device__ __forceinline__ unsigned pack2(float a, float b) {
  bf16x2 v; v[0] = (__bf16)a; v[1] = (__bf16)b;
  return __builtin_bit_cast(unsigned, v);
}

template <int NKB, int PB, int PA, int MT>
struct Sched {
  static constexpr int VB = 4, VA = 2 * MT;
  static constexpr int PM = PB > PA ? PB : PA;
  static constexpr int wait(int kb) {
    int pos = 0, endB = 0, endA = 0;
    for (int p = 0; p < PM; ++p) {
      if (p < PB && p < NKB) { pos += VB; if (p == kb) endB = pos; }
      if (p < PA && p < NKB) { pos += VA; if (p == kb) endA = pos; }
    }
    for (int it = 0; it < kb; ++it) {
      if (it + PB < NKB) { pos += VB; if (it + PB == kb) endB = pos; }
      if (it + PA < NKB) { pos += VA; if (it + PA == kb) endA = pos; }
    }
    int e = endB > endA ? endB : endA;
    return pos - e;
  }
};

__device__ __forceinline__ void glds16(unsigned voff, const void* sbase, unsigned lds_dst) {
  unsigned keep;
  asm volatile("s_mov_b32 %0, m0\n\ts_mov_b32 m0, %3\n\ts_nop 0\n\tglobal_load_lds_dwordx4 %1, %2\n\ts_mov_b32 m0, %0"
               : "=&s"(keep) : "v"(voff), "s"(sbase), "s"(lds_dst) : "memory");
}
__device__ __forceinline__ void glds16_half(unsigned voff, const void* sbase, unsigned lds_dst) {
  unsigned keep;
  asm volatile("s_mov_b32 %0, m0\n\ts_mov_b32 m0, %3\n\ts_mov_b32 exec_hi, 0\n\ts_nop 0\n\tglobal_load_lds_dwordx4 %1, %2\n\t"
               "s_mov_b32 exec_hi, -1\n\ts_mov_b32 m0, %0"
               : "=&s"(keep) : "v"(voff), "s"(sbase), "s"(lds_dst) : "memory");
}
template <int OFF>
__device__ __forceinline__ void gload16(bf16x8& dst, unsigned voff, const void* sbase) {
  asm volatile("global_load_dwordx4 %0, %1, %2 offset:%3" : "=v"(dst) : "v"(voff), "s"(sbase), "i"(OFF) : "memory");
}

__device__ __forceinline__ void tie2(bf16x8& x, bf16x8& y) { asm volatile("" : "+v"(x), "+v"(y)); }

template <int NKB, int NSLOT, int PA, int MT, int BKIND, bool SWAP>
__device__ __forceinline__ void gemm_core(const char* Abase,
                                          const unsigned (&avoff)[MT],
                                          const char* Bbase,
                                          int ldb2, char* smem, int wave, int lane, f32x4 (&acc)[MT][7]) {
  constexpr int PB = (NSLOT >= NKB) ? NKB : NSLOT - 1;
  using SC = Sched<NKB, PB, PA, MT>;
  const unsigned lds0 = (unsigned)(uintptr_t)(LDSP char*)smem;
  unsigned bvoff[4];
  unsigned bdst[4];
  static_for<4>([&](auto P) {
    constexpr int p = P;
    int L;
    if constexpr (p < 3) L = (wave * 3 + p) * 1024 + lane * 16;
    else L = 12 * 1024 + wave * 512 + (lane & 31) * 16;
    bdst[p] = (p < 3) ? (wave * 3 + p) * 1024 : 12 * 1024 + wave * 512;
    if constexpr (BKIND == 0) {
      int pos = L / 224, ch = (L - pos * 224) >> 4;
      int G = pos >> 5, e = pos & 3, g1 = (pos >> 2) & 1, q = (pos >> 3) & 1, i = (pos >> 4) & 1;
      int k = 32 * G + 16 * g1 + 8 * i + 4 * q + e;
      bvoff[p] = k * ldb2 + ch * 16;
    } else if constexpr (BKIND == 1) {
      int piece = L >> 10, l = (L >> 4) & 63;
      int i = piece / 7, nt = piece - i * 7;
      bvoff[p] = (16 * nt + (l & 15)) * ldb2 + 32 * (l >> 4) + 16 * i;
    } else {
      bvoff[p] = L;
    }
  });
  const int bstep = (BKIND == 0) ? 64 * ldb2 : (BKIND == 1 ? 128 : ldb2);

  bf16x8 a[PA + 1][MT][2];
  auto issueB = [&](auto KB) {
    constexpr int kb = KB;
    const char* src = Bbase + (ptrdiff_t)kb * bstep;
    const unsigned d = lds0 + (kb % NSLOT) * 14336;
    glds16(bvoff[0], src, d + bdst[0]);
    glds16(bvoff[1], src, d + bdst[1]);
    glds16(bvoff[2], src, d + bdst[2]);
    glds16_half(bvoff[3], src, d + bdst[3]);
  };
  auto issueA = [&](auto KB) {
    constexpr int kb = KB;
    static_for<MT>([&](auto M) {
      constexpr int m = M;
      gload16<kb * 128>(a[kb % (PA + 1)][m][0], avoff[m], Abase);
      gload16<kb * 128 + 16>(a[kb % (PA + 1)][m][1], avoff[m], Abase);
    });
  };
  static_for<SC::PM>([&](auto P) {
    constexpr int p = P;
    if constexpr (p < PB && p < NKB) issueB(P);
    if constexpr (p < PA && p < NKB) issueA(P);
  });
  int rbase;
  if constexpr (BKIND == 0) {
    int g = lane >> 4, e = (lane & 15) >> 2, pp = lane & 3;
    rbase = (32 * (g >> 1) + 4 * (g & 1) + e) * 224 + 8 * pp;
  } else {
    rbase = lane * 16;
  }
  static_for<NKB>([&](auto KB) {
    constexpr int kb = KB;
    constexpr int wN = SC::wait(kb);
    asm volatile("s_waitcnt vmcnt(%0) lgkmcnt(0)" ::"i"(wN) : "memory");
    __builtin_amdgcn_s_barrier();
    static_for<MT>([&](auto M) {
      constexpr int m = M;
      tie2(a[kb % (PA + 1)][m][0], a[kb % (PA + 1)][m][1]);
    });
    if constexpr (kb + PB < NKB && NSLOT < NKB) issueB(std::integral_constant<int, kb + PB>{});
    if constexpr (kb + PA < NKB) issueA(std::integral_constant<int, kb + PA>{});
    const char* sl = smem + (kb % NSLOT) * 14336 + rbase;
    static_for<2>([&](auto I) {
      constexpr int i = I;
      bf16x8 b[7];
      static_for<7>([&](auto N) {
        constexpr int nt = N;
        if constexpr (BKIND == 0) {
          s16x4 lo = __builtin_amdgcn_ds_read_tr16_b64_v4i16((LDSP s16x4*)(sl + (8 * (0 + 2 * i)) * 224 + 32 * nt));
          s16x4 hi = __builtin_amdgcn_ds_read_tr16_b64_v4i16((LDSP s16x4*)(sl + (8 * (1 + 2 * i)) * 224 + 32 * nt));
          typedef short s16x8 __attribute__((ext_vector_type(8)));
          s16x8 t = __builtin_shufflevector(lo, hi, 0, 1, 2, 3, 4, 5, 6, 7);
          b[nt] = __builtin_bit_cast(bf16x8, t);
        } else {
          b[nt] = *(const bf16x8*)(sl + (i * 7 + nt) * 1024);
        }
      });
      static_for<7>([&](auto N) {
        constexpr int nt = N;
        static_for<MT>([&](auto M) {
          constexpr int m = M;
          if constexpr (SWAP)
            acc[m][nt] = __builtin_amdgcn_mfma_f32_16x16x32_bf16(b[nt], a[kb % (PA + 1)][m][i], acc[m][nt], 0, 0, 0);
          else
            acc[m][nt] = __builtin_amdgcn_mfma_f32_16x16x32_bf16(a[kb % (PA + 1)][m][i], b[nt], acc[m][nt], 0, 0, 0);
        });
      });
    });
  });
}

constexpr int K1_MT = 3;
__global__ void __launch_bounds__(256, 1)
k1_proj(const __bf16* __restrict__ xa, const __bf16* __restrict__ wqkv, const __bf16* __restrict__ xb,
        const __bf16* __restrict__ whr, const __bf16* __restrict__ whi, char* __restrict__ ws) {
  extern __shared__ __attribute__((aligned(16))) char smem[];
  const int lane = threadIdx.x & 63;
  const int wave = __builtin_amdgcn_readfirstlane(threadIdx.x >> 6);
  const int bid = blockIdx.x;
  const int ct = bid % 20, rt = bid / 20;
  const int m0 = rt * 192 + wave * 48;
  const int c = lane & 15, g = lane >> 4;
  unsigned avoff[K1_MT];
#pragma unroll
  for (int m = 0; m < K1_MT; ++m) {
    int row = m0 + 16 * m + c;
    row = row < SEQ ? row : SEQ - 1;
    avoff[m] = row * (DM * 2) + 32 * g;
  }
  f32x4 acc[K1_MT][7];
#pragma unroll
  for (int m = 0; m < K1_MT; ++m)
#pragma unroll
    for (int n = 0; n < 7; ++n) acc[m][n] = f32x4{0.f, 0.f, 0.f, 0.f};

  if (ct < 8) {
    gemm_core<7, 7, 3, K1_MT, 0, true>((const char*)xa, avoff, (const char*)wqkv + ct * 224, 3 * DM * 2, smem, wave, lane, acc);
    const bool isq = ct < 4;
    const float sc = isq ? 0.18898223650461362f * 1.4426950408889634f : 1.0f;
    char* dst = ws + (isq ? WS_Q : WS_K);
    const unsigned padlo = isq ? 0u : 0x3f803f80u, padhi = isq ? 0u : 0x00003f80u;
#pragma unroll
    for (int m = 0; m < K1_MT; ++m) {
      const int tok = m0 + 16 * m + c;
      if (tok < SEQ) {
#pragma unroll
        for (int nt = 0; nt < 7; ++nt) {
          const int n4 = 4 * nt + g;
          const int hh = (n4 * 37) >> 8;
          const int d0 = 4 * (n4 - 7 * hh);
          const int head = 4 * (ct & 3) + hh;
          const int s = d0 >> 4, h = (d0 >> 3) & 1, jh = (d0 >> 2) & 1;
          char* p = dst + ((head * 64 + (tok >> 5)) * 2 + s) * 1024 + (h * 32 + (tok & 31)) * 16 + jh * 8;
          u32x2 v;
          v[0] = pack2(acc[m][nt][0] * sc, acc[m][nt][1] * sc);
          v[1] = pack2(acc[m][nt][2] * sc, acc[m][nt][3] * sc);
          *(u32x2*)p = v;
          if (d0 == 24) *(u32x2*)(p + 8) = u32x2{padlo, padhi};
        }
      }
    }
  } else if (ct < 12) {
    gemm_core<7, 7, 3, K1_MT, 0, false>((const char*)xa, avoff, (const char*)wqkv + ct * 224, 3 * DM * 2, smem, wave, lane, acc);
    char* dst = ws + WS_V;
#pragma unroll
    for (int m = 0; m < K1_MT; ++m) {
      const int t0 = m0 + 16 * m + 4 * g;
      if (t0 < SEQ) {
        const int kk = t0 & 31;
        const int s = kk >> 4, jh = (kk >> 3) & 1, h = (kk >> 2) & 1;
#pragma unroll
        for (int nt = 0; nt < 7; ++nt) {
          const int nl = 16 * nt + c;
          const int hh = (nl * 147) >> 12;
          const int d = nl - 28 * hh;
          const int head = 4 * (ct & 3) + hh;
          char* p = dst + ((head * 64 + (t0 >> 5)) * 2 + s) * 1024 + (h * 32 + d) * 16 + jh * 8;
          u32x2 v;
          v[0] = pack2(acc[m][nt][0], acc[m][nt][1]);
          v[1] = pack2(acc[m][nt][2], acc[m][nt][3]);
          *(u32x2*)p = v;
          if (d == 27) {
            *(u32x2*)(p + 16) = u32x2{0x3f803f80u, 0x3f803f80u};
            *(u32x2*)(p + 32) = u32x2{0u, 0u};
            *(u32x2*)(p + 48) = u32x2{0u, 0u};
            *(u32x2*)(p + 64) = u32x2{0u, 0u};
          }
        }
      }
    }
  } else {
    const bool isb = ct >= 16;
    const int nq = ct & 3;
    const char* W = (const char*)(isb ? whi : whr) + (size_t)nq * 112 * (DM * 2);
    gemm_core<7, 7, 3, K1_MT, 1, false>((const char*)xb, avoff, W, DM * 2, smem, wave, lane, acc);
    const float sg = isb ? -1.f : 1.f;
    char* dst = ws + WS_B;
#pragma unroll
    for (int m = 0; m < K1_MT; ++m) {
      const int t0 = m0 + 16 * m + 4 * g;
      if (t0 < SEQ) {
        const int kk = t0 + (isb ? SEQ : 0);
        const int kb = kk >> 6, gp = (kk >> 4) & 3, i = (kk >> 3) & 1, jh = (kk >> 2) & 1;
#pragma unroll
        for (int nt = 0; nt < 7; ++nt) {
          char* p = dst + (((kb * 4 + nq) * 2 + i) * 7 + nt) * 1024 + (gp * 16 + c) * 16 + jh * 8;
          u32x2 v;
          v[0] = pack2(acc[m][nt][0] * sg, acc[m][nt][1] * sg);
          v[1] = pack2(acc[m][nt][2] * sg, acc[m][nt][3] * sg);
          *(u32x2*)p = v;
        }
      }
    }
  }
}

__device__ __forceinline__ float xhalf_max(float v) {
  auto r = __builtin_amdgcn_permlane32_swap(__builtin_bit_cast(unsigned, v), __builtin_bit_cast(unsigned, v), false, false);
  return fmaxf(__builtin_bit_cast(float, (unsigned)r[0]), __builtin_bit_cast(float, (unsigned)r[1]));
}
__device__ __forceinline__ float tile_max(const f32x16& s) {
  float m = fmaxf(fmaxf(s[0], s[1]), s[2]);
#pragma unroll
  for (int i = 3; i < 15; i += 2) m = fmaxf(fmaxf(m, s[i]), s[i + 1]);
  m = fmaxf(m, s[15]);
  return xhalf_max(m);
}
__device__ __forceinline__ void exp_pack(const f32x16& s, bf16x8& p0, bf16x8& p1) {
#pragma unroll
  for (int j = 0; j < 8; ++j) {
    p0[j] = (__bf16)__builtin_amdgcn_exp2f(s[j]);
    p1[j] = (__bf16)__builtin_amdgcn_exp2f(s[8 + j]);
  }
}

__device__ __forceinline__ void attn_role(const char* __restrict__ ws, char* __restrict__ wso, char* smem, int wg, int wave, int lane) {
  const int xcd = wg & 7, idx = wg >> 3;
  const int head = xcd * 2 + (idx >> 4), qb = idx & 15;
  const bf16x8* Qf = (const bf16x8*)(ws + WS_Q) + (size_t)(head * 64 + qb * 4) * 128 + lane;
  const bf16x8* Kf = (const bf16x8*)(ws + WS_K) + (size_t)(head * 64 + wave * 16) * 128 + lane;
  const bf16x8* Vf = (const bf16x8*)(ws + WS_V) + (size_t)(head * 64 + wave * 16) * 128 + lane;
  const f32x16 zero16 = {0.f, 0.f, 0.f, 0.f, 0.f, 0.f, 0.f, 0.f, 0.f, 0.f, 0.f, 0.f, 0.f, 0.f, 0.f, 0.f};

  bf16x8 kf[3][2], vf[3][2];
  bf16x8 qf[4][2];
#pragma unroll
  for (int t = 0; t < 2; ++t) {
    kf[t][0] = Kf[t * 128]; kf[t][1] = Kf[t * 128 + 64];
  }
#pragma unroll
  for (int qt = 0; qt < 4; ++qt) { qf[qt][0] = Qf[qt * 128]; qf[qt][1] = Qf[qt * 128 + 64]; }
#pragma unroll
  for (int t = 0; t < 2; ++t) {
    vf[t][0] = Vf[t * 128]; vf[t][1] = Vf[t * 128 + 64];
  }
  const bool hi = lane >= 32;
  float mref[4];
#pragma unroll
  for (int qt = 0; qt < 4; ++qt) {
    f32x16 s = __builtin_amdgcn_mfma_f32_32x32x16_bf16(kf[0][0], qf[qt][0], zero16, 0, 0, 0);
    s = __builtin_amdgcn_mfma_f32_32x32x16_bf16(kf[0][1], qf[qt][1], s, 0, 0, 0);
    const float nm = -(tile_max(s) + 64.f);
    const __bf16 b0 = (__bf16)nm;
    const float r1 = nm - (float)b0;
    const __bf16 b1 = (__bf16)r1;
    const float r2 = r1 - (float)b1;
    const __bf16 b2 = (__bf16)r2;
    mref[qt] = -(((float)b0 + (float)b1) + (float)b2);
    if (hi) { qf[qt][1][4] = b0; qf[qt][1][5] = b1; qf[qt][1][6] = b2; }
  }
  f32x16 o[4];
#pragma unroll
  for (int qt = 0; qt < 4; ++qt) o[qt] = zero16;

  static_for<16>([&](auto KT) {
    constexpr int kt = KT;
    constexpr int cb = kt % 3, nb = (kt + 2) % 3;
    if constexpr (kt + 2 < 16) {
      kf[nb][0] = Kf[(kt + 2) * 128]; kf[nb][1] = Kf[(kt + 2) * 128 + 64];
      vf[nb][0] = Vf[(kt + 2) * 128]; vf[nb][1] = Vf[(kt + 2) * 128 + 64];
    }
#pragma unroll
    for (int qt = 0; qt < 4; ++qt) {
      f32x16 s = __builtin_amdgcn_mfma_f32_32x32x16_bf16(kf[cb][0], qf[qt][0], zero16, 0, 0, 0);
      s = __builtin_amdgcn_mfma_f32_32x32x16_bf16(kf[cb][1], qf[qt][1], s, 0, 0, 0);
      bf16x8 p0, p1;
      exp_pack(s, p0, p1);
      o[qt] = __builtin_amdgcn_mfma_f32_32x32x16_bf16(vf[cb][0], p0, o[qt], 0, 0, 0);
      o[qt] = __builtin_amdgcn_mfma_f32_32x32x16_bf16(vf[cb][1], p1, o[qt], 0, 0, 0);
    }
  });

  bool bad = false;
#pragma unroll
  for (int qt = 0; qt < 4; ++qt) {
    const float l = o[qt][12];
    bad |= hi && !(l >= 0x1p-80f && l <= 0x1p64f);
    float am = fabsf(o[qt][0]);
#pragma unroll
    for (int j = 1; j < 16; ++j) am = fmaxf(am, fabsf(o[qt][j]));
    float sm = 0.f;
#pragma unroll
    for (int j = 0; j < 16; ++j) sm += o[qt][j] * 0.f;
    bad |= !(am <= 0x1p120f) || !(sm == 0.f);
  }
  if (__any(bad)) {
#pragma unroll
    for (int qt = 0; qt < 4; ++qt) {
      if (hi) { qf[qt][1][4] = (__bf16)0.f; qf[qt][1][5] = (__bf16)0.f; qf[qt][1][6] = (__bf16)0.f; }
      o[qt] = zero16;
      mref[qt] = -INFINITY;
    }
#pragma unroll 1
    for (int kt = 0; kt < 16; ++kt) {
      const bf16x8 k0 = Kf[kt * 128], k1 = Kf[kt * 128 + 64], v0 = Vf[kt * 128], v1 = Vf[kt * 128 + 64];
#pragma unroll
      for (int qt = 0; qt < 4; ++qt) {
        f32x16 s = __builtin_amdgcn_mfma_f32_32x32x16_bf16(k0, qf[qt][0], zero16, 0, 0, 0);
        s = __builtin_amdgcn_mfma_f32_32x32x16_bf16(k1, qf[qt][1], s, 0, 0, 0);
        const float mn = fmaxf(mref[qt], tile_max(s));
        const float al = __builtin_amdgcn_exp2f(mref[qt] - mn);
        mref[qt] = mn;
#pragma unroll
        for (int j = 0; j < 16; ++j) { s[j] -= mn; o[qt][j] *= al; }
        bf16x8 p0, p1;
        exp_pack(s, p0, p1);
        o[qt] = __builtin_amdgcn_mfma_f32_32x32x16_bf16(v0, p0, o[qt], 0, 0, 0);
        o[qt] = __builtin_amdgcn_mfma_f32_32x32x16_bf16(v1, p1, o[qt], 0, 0, 0);
      }
    }
  }

  f32x4* part = (f32x4*)smem;
  float* mbuf = (float*)(smem + 65536);
#pragma unroll
  for (int qt = 0; qt < 4; ++qt) {
    mbuf[(qt * 4 + wave) * 64 + lane] = hi ? mref[qt] + __builtin_amdgcn_logf(o[qt][12]) : mref[qt];
    if (qt != wave) {
#pragma unroll
      for (int gq = 0; gq < 4; ++gq)
        part[((qt * 4 + wave) * 4 + gq) * 64 + lane] = f32x4{o[qt][4 * gq], o[qt][4 * gq + 1], o[qt][4 * gq + 2], o[qt][4 * gq + 3]};
    }
  }
  f32x16 mine = wave == 0 ? o[0] : (wave == 1 ? o[1] : (wave == 2 ? o[2] : o[3]));
  __syncthreads();
  float mw[4];
#pragma unroll
  for (int w = 0; w < 4; ++w) mw[w] = mbuf[(wave * 4 + w) * 64 + 32 + (lane & 31)];
  const float mm = fmaxf(fmaxf(mw[0], mw[1]), fmaxf(mw[2], mw[3]));
#pragma unroll
  for (int w = 0; w < 4; ++w) mw[w] = mbuf[(wave * 4 + w) * 64 + (lane & 31)];
  float r[16];
#pragma unroll
  for (int j = 0; j < 16; ++j) r[j] = 0.f;
#pragma unroll
  for (int w = 0; w < 4; ++w) {
    const float f = __builtin_amdgcn_exp2f(mw[w] - mm);
#pragma unroll
    for (int gq = 0; gq < 4; ++gq) {
      f32x4 t;
      if (w == wave) t = f32x4{mine[4 * gq], mine[4 * gq + 1], mine[4 * gq + 2], mine[4 * gq + 3]};
      else t = part[((wave * 4 + w) * 4 + gq) * 64 + lane];
#pragma unroll
      for (int e = 0; e < 4; ++e) r[4 * gq + e] += f * t[e];
    }
  }
  const float l = __shfl(r[12], (lane & 31) + 32);
  const float inv = 1.0f / l;
  const int tok = qb * 128 + wave * 32 + (lane & 31);
  char* op = wso + WS_O + (size_t)tok * (DM * 2) + head * (DH * 2) + (hi ? 8 : 0);
#pragma unroll
  for (int gq = 0; gq < 4; ++gq) {
    if (gq < 3 || !hi) {
      u32x2 v;
      v[0] = pack2(r[4 * gq] * inv, r[4 * gq + 1] * inv);
      v[1] = pack2(r[4 * gq + 2] * inv, r[4 * gq + 3] * inv);
      *(u32x2*)(op + gq * 16) = v;
    }
  }
}

__global__ void __launch_bounds__(256, 2)
k2_main(const __bf16* __restrict__ csr, const __bf16* __restrict__ csi, char* __restrict__ ws) {
  extern __shared__ __attribute__((aligned(16))) char smem[];
  const int lane = threadIdx.x & 63;
  const int wave = __builtin_amdgcn_readfirstlane(threadIdx.x >> 6);
  const int bid = blockIdx.x;
  if (bid < 256) {
    attn_role(ws, ws, smem, bid, wave, lane);
  } else {
    const int wg = bid - 256;
    const int xcd = wg & 7, idx = wg >> 3;
    const int ks = xcd >> 1, mt = (xcd & 1) * 8 + (idx >> 2), nq = idx & 3;
    const int c = lane & 15, g = lane >> 4;
    const int m0 = mt * 128 + wave * 32;
    unsigned avoff[2];
#pragma unroll
    for (int m = 0; m < 2; ++m) avoff[m] = (m0 + 16 * m + c) * (SEQ * 2) + 32 * g;
    const char* A = (const char*)(ks < 2 ? csr : csi) + (ks & 1) * 2048;
    const char* B = ws + WS_B + ((size_t)(ks * 16) * 4 + nq) * 14336;
    f32x4 acc[2][7];
#pragma unroll
    for (int m = 0; m < 2; ++m)
#pragma unroll
      for (int n = 0; n < 7; ++n) acc[m][n] = f32x4{0.f, 0.f, 0.f, 0.f};
    gemm_core<16, 5, 4, 2, 2, true>(A, avoff, B, 4 * 14336, smem, wave, lane, acc);
    f32x4* P = (f32x4*)(ws + WS_P) + ((size_t)ks * 128 * 28) * 64 + lane;
#pragma unroll
    for (int m = 0; m < 2; ++m)
#pragma unroll
      for (int n = 0; n < 7; ++n) P[(size_t)(((m0 >> 4) + m) * 28 + nq * 7 + n) * 64] = acc[m][n];
  }
}

__global__ void __launch_bounds__(256, 1)
k3_final(const __bf16* __restrict__ wo, const char* __restrict__ ws, __bf16* __restrict__ out) {
  extern __shared__ __attribute__((aligned(16))) char smem[];
  const int lane = threadIdx.x & 63;
  const int wave = __builtin_amdgcn_readfirstlane(threadIdx.x >> 6);
  const int bid = blockIdx.x;
  const int c = lane & 15, g = lane >> 4;
  if (bid < 128) {
    const int rt = bid >> 2, nq = bid & 3;
    const int m0 = rt * 64 + wave * 16;
    unsigned avoff[1] = {(unsigned)((m0 + c) * (DM * 2) + 32 * g)};
    f32x4 acc[1][7];
#pragma unroll
    for (int n = 0; n < 7; ++n) acc[0][n] = f32x4{0.f, 0.f, 0.f, 0.f};
    gemm_core<7, 7, 6, 1, 0, true>(ws + WS_O, avoff, (const char*)wo + nq * 224, DM * 2, smem, wave, lane, acc);
    char* op = (char*)out + (size_t)(m0 + c) * (2 * DM * 2) + (nq * 112 + 4 * g) * 2;
#pragma unroll
    for (int n = 0; n < 7; ++n) {
      u32x2 v;
      v[0] = pack2(acc[0][n][0], acc[0][n][1]);
      v[1] = pack2(acc[0][n][2], acc[0][n][3]);
      *(u32x2*)(op + n * 32) = v;
    }
  } else {
    const int mt = bid - 128;
    const f32x4* P = (const f32x4*)(ws + WS_P) + lane;
    f32x4 t[7][4];
#pragma unroll
    for (int n = 0; n < 7; ++n)
#pragma unroll
      for (int ks = 0; ks < 4; ++ks) t[n][ks] = P[((size_t)(ks * 128 + mt) * 28 + wave * 7 + n) * 64];
    char* op = (char*)out + (size_t)(mt * 16 + c) * (2 * DM * 2) + (DM + wave * 112 + 4 * g) * 2;
#pragma unroll
    for (int n = 0; n < 7; ++n) {
      f32x4 s = (t[n][0] + t[n][1]) + (t[n][2] + t[n][3]);
      u32x2 v;
      v[0] = pack2(s[0], s[1]);
      v[1] = pack2(s[2], s[3]);
      *(u32x2*)(op + n * 32) = v;
    }
  }
}

extern "C" void kernel_launch(void* const* d_in, const int* in_sizes, int n_in, void* d_out, int out_size,
                              void* d_ws, size_t ws_size, hipStream_t stream) {
  const __bf16* xa = (const __bf16*)d_in[0];
  const __bf16* wqkv = (const __bf16*)d_in[1];
  const __bf16* wo = (const __bf16*)d_in[2];
  const __bf16* xb = (const __bf16*)d_in[3];
  const __bf16* csr = (const __bf16*)d_in[4];
  const __bf16* csi = (const __bf16*)d_in[5];
  const __bf16* whr = (const __bf16*)d_in[6];
  const __bf16* whi = (const __bf16*)d_in[7];
  char* ws = (char*)d_ws;
  constexpr int L1 = 7 * 14336, L2 = 5 * 14336, L3 = 7 * 14336;
  (void)hipFuncSetAttribute((const void*)k1_proj, hipFuncAttributeMaxDynamicSharedMemorySize, L1);
  (void)hipFuncSetAttribute((const void*)k2_main, hipFuncAttributeMaxDynamicSharedMemorySize, L2);
  (void)hipFuncSetAttribute((const void*)k3_final, hipFuncAttributeMaxDynamicSharedMemorySize, L3);
  k1_proj<<<220, 256, L1, stream>>>(xa, wqkv, xb, whr, whi, ws);
  k2_main<<<512, 256, L2, stream>>>(csr, csi, ws);
  k3_final<<<256, 256, L3, stream>>>(wo, ws, (__bf16*)d_out);
}
```

```cpp
#include <hip/hip_runtime.h>
#include <hip/hip_bf16.h>
#include <stdint.h>
#include <utility>

typedef __bf16 bf16x8 __attribute__((ext_vector_type(8)));
typedef __bf16 bf16x4 __attribute__((ext_vector_type(4)));
typedef __bf16 bf16x2 __attribute__((ext_vector_type(2)));
typedef short s16x4 __attribute__((ext_vector_type(4)));
typedef float f32x4 __attribute__((ext_vector_type(4)));
typedef float f32x16 __attribute__((ext_vector_type(16)));
typedef unsigned u32x4 __attribute__((ext_vector_type(4)));
typedef unsigned u32x2 __attribute__((ext_vector_type(2)));
#define LDSP __attribute__((address_space(3)))

#define TSDECL
#define TS(i)
#define TSEND(wsb, kidx)

#define K3ROT ((2 * nq + (idx >> 2)) & 7)
#define K2ROT ((4 * nq + 2 * (idx >> 2)) & 15)
constexpr int SEQ = 2048, DM = 448, NH = 16, DH = 28;

constexpr size_t WS_Q = 0;
constexpr size_t WS_K = 2u << 20;
constexpr size_t WS_V = 4u << 20;
constexpr size_t WS_B = 6u << 20;
constexpr size_t WS_O = 10u << 20;
constexpr size_t WS_P = 12u << 20;

template <int... Is, class F>
__device__ __forceinline__ void static_for_impl(std::integer_sequence<int, Is...>, F&& f) {
  (f(std::integral_constant<int, Is>{}), ...);
}
template <int N, class F>
__device__ __forceinline__ void static_for(F&& f) {
  static_for_impl(std::make_integer_sequence<int, N>{}, static_cast<F&&>(f));
}

__device__ __forceinline__ unsigned pack2(float a, float b) {
  bf16x2 v; v[0] = (__bf16)a; v[1] = (__bf16)b;
  return __builtin_bit_cast(unsigned, v);
}

template <int NKB, int NSA, int NSB, int MT>
struct Sched {
  static constexpr int NS = 2 * NKB;
  static constexpr int VB = 2, VA = 2 * MT;
  static constexpr int PA = NSA >= NKB ? NKB : NSA - 1;
  static constexpr int PB = NSB >= NS ? NS : NSB - 1;
  static constexpr int PM = (PA > (PB + 1) / 2) ? PA : (PB + 1) / 2;
  static constexpr int wait(int s) {
    int pos = 0, endB = 0, endA = 0;
    for (int p = 0; p < PM; ++p) {
      if (p < PA) { pos += VA; if (p == s / 2) endA = pos; }
      if (2 * p < PB) { pos += VB; if (2 * p == s) endB = pos; }
      if (2 * p + 1 < PB) { pos += VB; if (2 * p + 1 == s) endB = pos; }
    }
    for (int it = 0; it < s; ++it) {
      if (it + PB < NS) { pos += VB; if (it + PB == s) endB = pos; }
      if (it / 2 + PA < NKB) { pos += MT; if (it / 2 + PA == s / 2) endA = pos; }
    }
    int e = endB > endA ? endB : endA;
    return pos - e;
  }
};

__device__ __forceinline__ void glds16(unsigned voff, const void* sbase, unsigned lds_dst) {
  unsigned keep;
  asm volatile("s_mov_b32 %0, m0\n\ts_mov_b32 m0, %3\n\ts_nop 0\n\tglobal_load_lds_dwordx4 %1, %2\n\ts_mov_b32 m0, %0"
               : "=&s"(keep) : "v"(voff), "s"(sbase), "s"(lds_dst) : "memory");
}
__device__ __forceinline__ void glds16_48(unsigned voff, const void* sbase, unsigned lds_dst) {
  unsigned keep;
  asm volatile("s_mov_b32 %0, m0\n\ts_mov_b32 m0, %3\n\ts_mov_b32 exec_hi, 0xffff\n\ts_nop 0\n\tglobal_load_lds_dwordx4 %1, %2\n\t"
               "s_mov_b32 exec_hi, -1\n\ts_mov_b32 m0, %0"
               : "=&s"(keep) : "v"(voff), "s"(sbase), "s"(lds_dst) : "memory");
}

template <int OFF>
__device__ __forceinline__ void gload16(bf16x8& dst, unsigned voff, const void* sbase) {
  asm volatile("global_load_dwordx4 %0, %1, %2 offset:%3" : "=v"(dst) : "v"(voff), "s"(sbase), "i"(OFF) : "memory");
}
__device__ __forceinline__ void tie2(bf16x8& x, bf16x8& y) { asm volatile("" : "+v"(x), "+v"(y)); }

template <int NKB, int NSA, int NSB, int MT, int BKIND, bool SWAP>
__device__ __forceinline__ void gemm_core(const char* Abase, const unsigned (&avoff)[2 * MT], const char* Bbase, int ldb2,
                                          char* smem, int wave, int lane, int rot, f32x4 (&acc)[MT][7]) {
  using SC = Sched<NKB, NSA, NSB, MT>;
  constexpr int NS = SC::NS, PA = SC::PA, PB = SC::PB;
  constexpr int ASLOT = MT * 8192;
  constexpr bool BTR = BKIND == 0 || BKIND == 3, ADIR = BKIND == 3;
  constexpr int BOFF = ADIR ? 0 : NSA * ASLOT;
  const unsigned lds0 = (unsigned)(uintptr_t)(LDSP char*)smem;
  unsigned bvoff[2];
  static_for<2>([&](auto P) {
    constexpr int p = P;
    int L = wave * 1792 + p * 1024 + lane * 16;
    if (p == 1 && lane >= 48) L = wave * 1792;
    if constexpr (BTR) {
      int pos = L / 224, ch = (L - pos * 224) >> 4;
      int k = (pos & 3) + 8 * ((pos >> 2) & 1) + 4 * ((pos >> 3) & 1) + (pos & 16);
      if constexpr (BKIND == 3) k = k < 28 ? k : 27;
      bvoff[p] = k * ldb2 + ch * 16;
    } else if constexpr (BKIND == 1) {
      int n = L >> 6, chp = (L >> 4) & 3;
      bvoff[p] = n * ldb2 + 16 * (chp ^ ((-(n >> 2)) & 3));
    } else {
      bvoff[p] = L;
    }
  });
  const int bstep = (BKIND == 0) ? 32 * ldb2 : (BKIND == 1 ? 64 : (BKIND == 2 ? ldb2 : 28 * ldb2));
  bf16x8 fd[ADIR ? NS : 1][MT];
  auto issueB1 = [&](auto S, auto P) {
    constexpr int s = S;
    int se = s + 2 * rot;
    se = se >= NS ? se - NS : se;
    const char* src = Bbase + (ptrdiff_t)se * bstep;
    const unsigned d = lds0 + BOFF + (s % NSB) * 7168 + wave * 1792;
    if constexpr (P == 0) glds16(bvoff[0], src, d);
    else glds16_48(bvoff[1], src, d + 1024);
  };
  auto issueA1 = [&](auto KB, auto P) {
    constexpr int kb = KB;
    int ke = kb + rot;
    ke = ke >= NKB ? ke - NKB : ke;
    if constexpr (ADIR) {
      gload16<(P & 1) * 1024>(fd[2 * kb + (P & 1)][P >> 1], avoff[P & ~1], Abase + ke * 2048);
    } else {
      const char* src = Abase + ke * 128;
      const unsigned d = lds0 + (kb % NSA) * ASLOT + wave * (MT * 2048);
      glds16(avoff[P], src, d + P * 1024);
    }
  };
  auto issueB = [&](auto S) { static_for<2>([&](auto P) { issueB1(S, P); }); };
  auto issueA = [&](auto KB) { static_for<2 * MT>([&](auto P) { issueA1(KB, P); }); };
  auto stepload = [&](auto S, auto J) {
    constexpr int s = S, j = J;
    if constexpr (j < 2) {
      if constexpr (s + PB < NS) issueB1(std::integral_constant<int, s + PB>{}, J);
    } else {
      if constexpr (s / 2 + PA < NKB)
        issueA1(std::integral_constant<int, s / 2 + PA>{}, std::integral_constant<int, (s & 1) * MT + j - 2>{});
    }
  };
  static_for<SC::PM>([&](auto P) {
    constexpr int p = P;
    if constexpr (p < PA) issueA(P);
    if constexpr (2 * p < PB) issueB(std::integral_constant<int, 2 * p>{});
    if constexpr (2 * p + 1 < PB) issueB(std::integral_constant<int, 2 * p + 1>{});
  });
  const int c = lane & 15, g = lane >> 4;
  int rb;
  if constexpr (BTR) rb = ((c >> 2) + 4 * (g & 1) + 16 * (g >> 1)) * 224 + 8 * (c & 3);
  else if constexpr (BKIND == 1) rb = c * 64 + 16 * (g ^ ((-(c >> 2)) & 3));
  else rb = lane * 16;
  const int ra0 = wave * (MT * 2048) + c * 128 + 16 * (g ^ (c >> 1));
  const int ra1 = ra0 ^ 64;

  bf16x8 fa[2][MT], fb[2][7];
  auto sync = [&](auto S) {
    constexpr int s = S;
    asm volatile("s_waitcnt vmcnt(%0) lgkmcnt(0)" ::"i"(SC::wait(s)) : "memory");
    __builtin_amdgcn_s_barrier();
    if constexpr (ADIR && (s & 1) == 0) static_for<MT>([&](auto M) { tie2(fd[s][M], fd[s + 1][M]); });
    __builtin_amdgcn_sched_barrier(0);
  };
  constexpr int NRA = ADIR ? 0 : MT;
  constexpr int NSIDE = NRA + 7 + 2 + MT;
  auto side = [&](auto S, auto K) {
    constexpr int s = S, k = K;
    if constexpr (k < NRA) {
      const char* sa = smem + ((s / 2) % NSA) * ASLOT + ((s & 1) ? ra1 : ra0);
      fa[s & 1][k] = *(const bf16x8*)(sa + k * 2048);
    } else if constexpr (k < NRA + 7) {
      constexpr int nt = k - NRA;
      const char* sb = smem + BOFF + (s % NSB) * 7168 + rb;
      if constexpr (BTR) {
        s16x4 lo = __builtin_amdgcn_ds_read_tr16_b64_v4i16((LDSP s16x4*)(sb + 32 * nt));
        s16x4 hi = __builtin_amdgcn_ds_read_tr16_b64_v4i16((LDSP s16x4*)(sb + 8 * 224 + 32 * nt));
        typedef short s16x8 __attribute__((ext_vector_type(8)));
        s16x8 t = __builtin_shufflevector(lo, hi, 0, 1, 2, 3, 4, 5, 6, 7);
        fb[s & 1][nt] = __builtin_bit_cast(bf16x8, t);
      } else {
        fb[s & 1][nt] = *(const bf16x8*)(sb + nt * 1024);
      }
    } else {
      stepload(S, std::integral_constant<int, k - NRA - 7>{});
    }
    __builtin_amdgcn_sched_barrier(0);
  };
  sync(std::integral_constant<int, 0>{});
  static_for<NSIDE>([&](auto K) { side(std::integral_constant<int, 0>{}, K); });
  static_for<NS>([&](auto S) {
    constexpr int s = S;
    if constexpr (s + 1 < NS) sync(std::integral_constant<int, s + 1>{});
    static_for<7>([&](auto N) {
      constexpr int nt = N;
      static_for<MT>([&](auto M) {
        constexpr int m = M;
        const bf16x8 av = ADIR ? fd[ADIR ? s : 0][m] : fa[s & 1][m];
        if constexpr (SWAP)
          acc[m][nt] = __builtin_amdgcn_mfma_f32_16x16x32_bf16(fb[s & 1][nt], av, acc[m][nt], 0, 0, 0);
        else
          acc[m][nt] = __builtin_amdgcn_mfma_f32_16x16x32_bf16(av, fb[s & 1][nt], acc[m][nt], 0, 0, 0);
        __builtin_amdgcn_sched_barrier(0);
        if constexpr (s + 1 < NS) {
          static_for<NSIDE>([&](auto K) {
            if constexpr ((K * 7 * MT) / NSIDE == nt * MT + m) side(std::integral_constant<int, s + 1>{}, K);
          });
        }
      });
    });
  });
}

template <int MT>
__device__ __forceinline__ void a_offsets(unsigned (&avoff)[2 * MT], int m0, int lda2, int lane, int maxrow) {
#pragma unroll
  for (int p = 0; p < 2 * MT; ++p) {
    const int r = 8 * p + (lane >> 3);
    int row = m0 + r;
    row = row < maxrow ? row : maxrow;
    avoff[p] = row * lda2 + 16 * ((lane & 7) ^ ((r >> 1) & 7));
  }
}

template <int MT>
__device__ __forceinline__ void store_rows(const f32x4 (&acc)[MT][7], char* tb, int tstride, char* dst, int ld2, int lane) {
  const int c = lane & 15, g = lane >> 4;
#pragma unroll
  for (int m = 0; m < MT; ++m)
#pragma unroll
    for (int n = 0; n < 7; ++n)
      *(u32x2*)(tb + m * tstride + c * 240 + n * 32 + g * 8) = u32x2{pack2(acc[m][n][0], acc[m][n][1]), pack2(acc[m][n][2], acc[m][n][3])};
#pragma unroll
  for (int m = 0; m < MT; ++m)
#pragma unroll
    for (int i = 0; i < 4; ++i) {
      const int u = i * 64 + lane;
      const int row = (u * 4682) >> 16, ch = u - row * 14;
      if (i < 3 || lane < 32) {
        const u32x4 v = *(const u32x4*)(tb + m * tstride + row * 240 + ch * 16);
        *(u32x4*)(dst + (size_t)(m * 16 + row) * ld2 + ch * 16) = v;
      }
    }
}

constexpr int K1_MT = 3;
#define K1CFG 7, 4, 9
__global__ void __launch_bounds__(256, 1)
k1_proj(const __bf16* __restrict__ xa, const __bf16* __restrict__ wqkv, const __bf16* __restrict__ xb,
        const __bf16* __restrict__ whr, const __bf16* __restrict__ whi, char* __restrict__ ws) {
  extern __shared__ __attribute__((aligned(16))) char smem[];
  const int lane = threadIdx.x & 63;
  const int wave = __builtin_amdgcn_readfirstlane(threadIdx.x >> 6);
  const int bid = blockIdx.x;
  TSDECL TS(0); TS(1);
  const int xcd = bid & 7, idx = bid >> 3;
  const int rt = idx / 10, ci = idx - 10 * rt;
  const int ct = (xcd & 1) * 10 + ci;
  const int lim = (xcd >> 1) * 512 + 512;
  const int m0 = (xcd >> 1) * 512 + rt * 192 + wave * 48;
  const int rot = (ci + 3 * rt) % 7;
  const int c = lane & 15, g = lane >> 4;
  unsigned avoff[2 * K1_MT];
  a_offsets<K1_MT>(avoff, m0, DM * 2, lane, lim - 1);
  f32x4 acc[K1_MT][7];
#pragma unroll
  for (int m = 0; m < K1_MT; ++m)
#pragma unroll
    for (int n = 0; n < 7; ++n) acc[m][n] = f32x4{0.f, 0.f, 0.f, 0.f};

  char* tb = smem + wave * 6144;
  if (ct < 8) {
    gemm_core<K1CFG, K1_MT, 0, true>((const char*)xa, avoff, (const char*)wqkv + ct * 224, 3 * DM * 2, smem, wave, lane, rot, acc);
    TS(2);
    const bool isq = ct < 4;
    const float sc = isq ? 0.18898223650461362f * 1.4426950408889634f : 1.0f;
    char* dst = ws + (isq ? WS_Q : WS_K);
    const unsigned padlo = isq ? 0u : 0x3f803f80u, padhi = isq ? 0u : 0x00003f80u;
#pragma unroll
    for (int m = 0; m < K1_MT; ++m) {
      char* st = tb + m * 24576;
#pragma unroll
      for (int nt = 0; nt < 7; ++nt) {
        const int n4 = 4 * nt + g;
        const int hh = (n4 * 37) >> 8;
        const int q4 = n4 - 7 * hh;
        char* p = st + (hh * 4 + (q4 >> 1)) * 256 + c * 16 + (q4 & 1) * 8;
        *(u32x2*)p = u32x2{pack2(acc[m][nt][0] * sc, acc[m][nt][1] * sc), pack2(acc[m][nt][2] * sc, acc[m][nt][3] * sc)};
        if (q4 == 6) *(u32x2*)(p + 8) = u32x2{padlo, padhi};
      }
    }
#pragma unroll
    for (int m = 0; m < K1_MT; ++m) {
      const int t0 = m0 + 16 * m;
      if (t0 < lim) {
#pragma unroll
        for (int hh = 0; hh < 4; ++hh) {
          const u32x4 v = *(const u32x4*)(tb + m * 24576 + hh * 1024 + lane * 16);
          *(u32x4*)(dst + ((4 * (ct & 3) + hh) * 64 + (t0 >> 5)) * 2048 + g * 512 + ((t0 & 16) + c) * 16) = v;
        }
      }
    }
  } else if (ct < 12) {
    gemm_core<K1CFG, K1_MT, 0, false>((const char*)xa, avoff, (const char*)wqkv + ct * 224, 3 * DM * 2, smem, wave, lane, rot, acc);
    TS(2);
    char* dst = ws + WS_V;
#pragma unroll
    for (int m = 0; m < K1_MT; ++m) {
      char* st = tb + m * 24576;
#pragma unroll
      for (int nt = 0; nt < 7; ++nt) {
        const int nl = 16 * nt + c;
        const int hh = (nl * 147) >> 12;
        const int d = nl - 28 * hh;
        char* p = st + ((hh * 2 + (g & 1)) * 32 + d) * 16 + (g >> 1) * 8;
        *(u32x2*)p = u32x2{pack2(acc[m][nt][0], acc[m][nt][1]), pack2(acc[m][nt][2], acc[m][nt][3])};
      }
      if (lane < 32) {
        const unsigned o1 = (lane & 3) == 0 ? 0x3f803f80u : 0u;
        *(u32x4*)(st + ((lane >> 2) * 32 + 28 + (lane & 3)) * 16) = u32x4{o1, o1, o1, o1};
      }
    }
#pragma unroll
    for (int m = 0; m < K1_MT; ++m) {
      const int t0 = m0 + 16 * m;
      if (t0 < lim) {
#pragma unroll
        for (int hh = 0; hh < 4; ++hh) {
          const u32x4 v = *(const u32x4*)(tb + m * 24576 + hh * 1024 + lane * 16);
          *(u32x4*)(dst + ((4 * (ct & 3) + hh) * 128 + (t0 >> 4)) * 1024 + lane * 16) = v;
        }
      }
    }
  } else {
    const bool isb = ct >= 16;
    const int nq = ct & 3;
    const char* W = (const char*)(isb ? whi : whr) + (size_t)nq * 112 * (DM * 2);
    gemm_core<K1CFG, K1_MT, 1, false>((const char*)xb, avoff, W, DM * 2, smem, wave, lane, rot, acc);
    TS(2);
    const float sg = isb ? -1.f : 1.f;
    char* dst = ws + WS_B;
#pragma unroll
    for (int m = 0; m < K1_MT; ++m) {
      char* st = tb + m * 24576;
#pragma unroll
      for (int nt = 0; nt < 7; ++nt)
        *(u32x2*)(st + nt * 512 + (g >> 1) * 256 + c * 16 + (g & 1) * 8) =
            u32x2{pack2(acc[m][nt][0] * sg, acc[m][nt][1] * sg), pack2(acc[m][nt][2] * sg, acc[m][nt][3] * sg)};
    }
#pragma unroll
    for (int m = 0; m < K1_MT; ++m) {
      const int t0 = m0 + 16 * m;
      if (t0 < lim) {
        const int kk = t0 + (isb ? SEQ : 0);
        char* gp = dst + ((kk >> 5) * 4 + nq) * 7168 + (kk & 16) * 32 + (lane >> 5) * 1024 + (lane & 31) * 16;
#pragma unroll
        for (int i = 0; i < 4; ++i) {
          if (i < 3 || lane < 32) {
            const u32x4 v = *(const u32x4*)(tb + m * 24576 + i * 1024 + lane * 16);
            *(u32x4*)(gp + i * 2048) = v;
          }
        }
      }
    }
  }
  TSEND(ws, 0);
}

__device__ __forceinline__ float xhalf_max(float v) {
  auto r = __builtin_amdgcn_permlane32_swap(__builtin_bit_cast(unsigned, v), __builtin_bit_cast(unsigned, v), false, false);
  return fmaxf(__builtin_bit_cast(float, (unsigned)r[0]), __builtin_bit_cast(float, (unsigned)r[1]));
}
__device__ __forceinline__ float tile_max(const f32x16& s) {
  float m = fmaxf(fmaxf(s[0], s[1]), s[2]);
#pragma unroll
  for (int i = 3; i < 15; i += 2) m = fmaxf(fmaxf(m, s[i]), s[i + 1]);
  m = fmaxf(m, s[15]);
  return xhalf_max(m);
}
__device__ __forceinline__ void exp_pack(const f32x16& s, bf16x8& p0, bf16x8& p1) {
#pragma unroll
  for (int j = 0; j < 8; ++j) {
    p0[j] = (__bf16)__builtin_amdgcn_exp2f(s[j]);
    p1[j] = (__bf16)__builtin_amdgcn_exp2f(s[8 + j]);
  }
}

__device__ __forceinline__ void attn_role(const char* __restrict__ ws, char* __restrict__ wso, char* smem, int wg, int wave, int lane) {
  TSDECL TS(0);
  const int xcd = wg & 7, idx = wg >> 3;
  const int head = xcd * 2 + (idx >> 4), qb = idx & 15;
  const bf16x8* Qf = (const bf16x8*)(ws + WS_Q) + (size_t)(head * 64 + qb * 4) * 128 + lane;
  const bf16x8* Kf = (const bf16x8*)(ws + WS_K) + (size_t)(head * 64 + wave * 16) * 128 + lane;
  const bf16x8* Vf = (const bf16x8*)(ws + WS_V) + (size_t)(head * 64 + wave * 16) * 128 + lane;
  const f32x16 zero16 = {0.f, 0.f, 0.f, 0.f, 0.f, 0.f, 0.f, 0.f, 0.f, 0.f, 0.f, 0.f, 0.f, 0.f, 0.f, 0.f};

  const char* Kb = (const char*)(ws + WS_K) + (size_t)(head * 64 + wave * 16) * 2048;
  const unsigned l16 = lane * 16;
  bf16x8 kf[3][2], vf[3][2];
  bf16x8 qf[4][2];
  auto loadkv = [&](auto T) {
    constexpr int t = T;
    const char* src = Kb + ((t + qb) & 15) * 2048;
    gload16<0>(kf[t % 3][0], l16, src);
    gload16<1024>(kf[t % 3][1], l16, src);
    gload16<0>(vf[t % 3][0], l16, src + (WS_V - WS_K));
    gload16<1024>(vf[t % 3][1], l16, src + (WS_V - WS_K));
  };
  loadkv(std::integral_constant<int, 0>{});
#pragma unroll
  for (int qt = 0; qt < 4; ++qt) { qf[qt][0] = Qf[qt * 128]; qf[qt][1] = Qf[qt * 128 + 64]; }
  asm volatile("s_waitcnt vmcnt(0)" ::: "memory");
  tie2(kf[0][0], kf[0][1]); tie2(vf[0][0], vf[0][1]);
#pragma unroll
  for (int qt = 0; qt < 4; ++qt) tie2(qf[qt][0], qf[qt][1]);
  loadkv(std::integral_constant<int, 1>{});
  const bool hi = lane >= 32;
  float mref[4];
#pragma unroll
  for (int qt = 0; qt < 4; ++qt) {
    f32x16 s = __builtin_amdgcn_mfma_f32_32x32x16_bf16(kf[0][0], qf[qt][0], zero16, 0, 0, 0);
    s = __builtin_amdgcn_mfma_f32_32x32x16_bf16(kf[0][1], qf[qt][1], s, 0, 0, 0);
    const float nm = -(tile_max(s) + 64.f);
    const __bf16 b0 = (__bf16)nm;
    const float r1 = nm - (float)b0;
    const __bf16 b1 = (__bf16)r1;
    const float r2 = r1 - (float)b1;
    const __bf16 b2 = (__bf16)r2;
    mref[qt] = -(((float)b0 + (float)b1) + (float)b2);
    if (hi) { qf[qt][1][4] = b0; qf[qt][1][5] = b1; qf[qt][1][6] = b2; }
  }
  TS(1);
  f32x16 o[4];
#pragma unroll
  for (int qt = 0; qt < 4; ++qt) o[qt] = zero16;

  {
#define SB() __builtin_amdgcn_sched_barrier(0)
    f32x16 sc[2];
    u32x4 pa, pb;
    float ea[4], eb[4];
    sc[0] = __builtin_amdgcn_mfma_f32_32x32x16_bf16(kf[0][0], qf[0][0], zero16, 0, 0, 0);
    sc[0] = __builtin_amdgcn_mfma_f32_32x32x16_bf16(kf[0][1], qf[0][1], sc[0], 0, 0, 0);
    SB();
    static_for<64>([&](auto I) {
      constexpr int i = I, kt = i >> 2, qt = i & 3;
      constexpr int ni = i + 1, nkt = ni >> 2, nqt = ni & 3;
      constexpr int pi = i - 1, pkt = pi >> 2, pqt = pi & 3;
      const f32x16& cur = sc[i & 1];
      if constexpr (qt == 3 && ni < 64) {
        asm volatile("s_waitcnt vmcnt(%0)" ::"i"(kt + 2 < 16 ? 4 : 0) : "memory");
        tie2(kf[nkt % 3][0], kf[nkt % 3][1]); tie2(vf[nkt % 3][0], vf[nkt % 3][1]);
      }
      if constexpr (ni < 64) sc[ni & 1] = __builtin_amdgcn_mfma_f32_32x32x16_bf16(kf[nkt % 3][0], qf[nqt][0], zero16, 0, 0, 0);
      SB();
      if constexpr (i > 0) { pb[2] = pack2(eb[0], eb[1]); pb[3] = pack2(eb[2], eb[3]); }
      SB();
#pragma unroll
      for (int j = 0; j < 4; ++j) ea[j] = __builtin_amdgcn_exp2f(cur[j]);
      SB();
      if constexpr (i > 0) o[pqt] = __builtin_amdgcn_mfma_f32_32x32x16_bf16(vf[pkt % 3][1], __builtin_bit_cast(bf16x8, pb), o[pqt], 0, 0, 0);
      SB();
      pa[0] = pack2(ea[0], ea[1]); pa[1] = pack2(ea[2], ea[3]);
      SB();
#pragma unroll
      for (int j = 0; j < 4; ++j) eb[j] = __builtin_amdgcn_exp2f(cur[4 + j]);
      if constexpr (qt == 0 && kt + 2 < 16) loadkv(std::integral_constant<int, kt + 2>{});
      SB();
      if constexpr (ni < 64) sc[ni & 1] = __builtin_amdgcn_mfma_f32_32x32x16_bf16(kf[nkt % 3][1], qf[nqt][1], sc[ni & 1], 0, 0, 0);
      SB();
      pa[2] = pack2(eb[0], eb[1]); pa[3] = pack2(eb[2], eb[3]);
      SB();
#pragma unroll
      for (int j = 0; j < 4; ++j) ea[j] = __builtin_amdgcn_exp2f(cur[8 + j]);
      SB();
      o[qt] = __builtin_amdgcn_mfma_f32_32x32x16_bf16(vf[kt % 3][0], __builtin_bit_cast(bf16x8, pa), o[qt], 0, 0, 0);
      SB();
      pb[0] = pack2(ea[0], ea[1]); pb[1] = pack2(ea[2], ea[3]);
      SB();
#pragma unroll
      for (int j = 0; j < 4; ++j) eb[j] = __builtin_amdgcn_exp2f(cur[12 + j]);
      SB();
    });
    pb[2] = pack2(eb[0], eb[1]); pb[3] = pack2(eb[2], eb[3]);
    o[3] = __builtin_amdgcn_mfma_f32_32x32x16_bf16(vf[15 % 3][1], __builtin_bit_cast(bf16x8, pb), o[3], 0, 0, 0);
  }

  TS(2);
  bool bad = false;
#pragma unroll
  for (int qt = 0; qt < 4; ++qt) {
    const float l = o[qt][12];
    bad |= hi && !(l >= 0x1p-80f && l <= 0x1p64f);
    float am = fabsf(o[qt][0]);
#pragma unroll
    for (int j = 1; j < 16; ++j) am += fabsf(o[qt][j]);
    bad |= !(am <= 0x1p120f);
  }
  if (__any(bad)) {
#pragma unroll
    for (int qt = 0; qt < 4; ++qt) {
      if (hi) { qf[qt][1][4] = (__bf16)0.f; qf[qt][1][5] = (__bf16)0.f; qf[qt][1][6] = (__bf16)0.f; }
      o[qt] = zero16;
      mref[qt] = -INFINITY;
    }
#pragma unroll 1
    for (int kt = 0; kt < 16; ++kt) {
      const bf16x8 k0 = Kf[kt * 128], k1 = Kf[kt * 128 + 64], v0 = Vf[kt * 128], v1 = Vf[kt * 128 + 64];
#pragma unroll
      for (int qt = 0; qt < 4; ++qt) {
        f32x16 s = __builtin_amdgcn_mfma_f32_32x32x16_bf16(k0, qf[qt][0], zero16, 0, 0, 0);
        s = __builtin_amdgcn_mfma_f32_32x32x16_bf16(k1, qf[qt][1], s, 0, 0, 0);
        const float mn = fmaxf(mref[qt], tile_max(s));
        const float al = __builtin_amdgcn_exp2f(mref[qt] - mn);
        mref[qt] = mn;
#pragma unroll
        for (int j = 0; j < 16; ++j) { s[j] -= mn; o[qt][j] *= al; }
        bf16x8 p0, p1;
        exp_pack(s, p0, p1);
        o[qt] = __builtin_amdgcn_mfma_f32_32x32x16_bf16(v0, p0, o[qt], 0, 0, 0);
        o[qt] = __builtin_amdgcn_mfma_f32_32x32x16_bf16(v1, p1, o[qt], 0, 0, 0);
      }
    }
  }

  f32x4* part = (f32x4*)smem;
  float* mbuf = (float*)(smem + 65536);
#pragma unroll
  for (int qt = 0; qt < 4; ++qt) {
    mbuf[(qt * 4 + wave) * 64 + lane] = hi ? mref[qt] + __builtin_amdgcn_logf(o[qt][12]) : mref[qt];
    if (qt != wave) {
#pragma unroll
      for (int gq = 0; gq < 4; ++gq)
        part[((qt * 4 + wave) * 4 + gq) * 64 + lane] = f32x4{o[qt][4 * gq], o[qt][4 * gq + 1], o[qt][4 * gq + 2], o[qt][4 * gq + 3]};
    }
  }
  f32x16 mine = wave == 0 ? o[0] : (wave == 1 ? o[1] : (wave == 2 ? o[2] : o[3]));
  __syncthreads();
  float mw[4];
#pragma unroll
  for (int w = 0; w < 4; ++w) mw[w] = mbuf[(wave * 4 + w) * 64 + 32 + (lane & 31)];
  const float mm = fmaxf(fmaxf(mw[0], mw[1]), fmaxf(mw[2], mw[3]));
#pragma unroll
  for (int w = 0; w < 4; ++w) mw[w] = mbuf[(wave * 4 + w) * 64 + (lane & 31)];
  float r[16];
#pragma unroll
  for (int j = 0; j < 16; ++j) r[j] = 0.f;
#pragma unroll
  for (int w = 0; w < 4; ++w) {
    const float f = __builtin_amdgcn_exp2f(mw[w] - mm);
#pragma unroll
    for (int gq = 0; gq < 4; ++gq) {
      f32x4 t;
      if (w == wave) t = f32x4{mine[4 * gq], mine[4 * gq + 1], mine[4 * gq + 2], mine[4 * gq + 3]};
      else t = part[((wave * 4 + w) * 4 + gq) * 64 + lane];
#pragma unroll
      for (int e = 0; e < 4; ++e) r[4 * gq + e] += f * t[e];
    }
  }
  const float l = __shfl(r[12], (lane & 31) + 32);
  const float inv = 1.0f / l;
  unsigned w[8];
#pragma unroll
  for (int gq = 0; gq < 4; ++gq) {
    w[2 * gq] = pack2(r[4 * gq] * inv, r[4 * gq + 1] * inv);
    w[2 * gq + 1] = pack2(r[4 * gq + 2] * inv, r[4 * gq + 3] * inv);
  }
  if (hi) { w[6] = 0u; w[7] = 0u; }
  char* op = wso + WS_O + ((size_t)(qb * 8 + wave * 2 + ((lane >> 4) & 1)) * 16 + head) * 1024 + (hi ? 256 : 0) + (lane & 15) * 16;
#pragma unroll
  for (int pr = 0; pr < 2; ++pr) {
    auto s0 = __builtin_amdgcn_permlane32_swap(w[4 * pr], w[4 * pr + 2], false, false);
    auto s1 = __builtin_amdgcn_permlane32_swap(w[4 * pr + 1], w[4 * pr + 3], false, false);
    *(u32x4*)(op + pr * 512) = u32x4{(unsigned)s0[0], (unsigned)s1[0], (unsigned)s0[1], (unsigned)s1[1]};
  }
  TSEND(wso, 1);
}

__global__ void __launch_bounds__(256, 2)
k2_main(const __bf16* __restrict__ csr, const __bf16* __restrict__ csi, char* __restrict__ ws) {
  extern __shared__ __attribute__((aligned(16))) char smem[];
  const int lane = threadIdx.x & 63;
  const int wave = __builtin_amdgcn_readfirstlane(threadIdx.x >> 6);
  const int bid = blockIdx.x;
  if (bid < 256) {
    attn_role(ws, ws, smem, bid, wave, lane);
  } else {
    TSDECL TS(0); TS(1);
    const int wg = bid - 256;
    const int xcd = wg & 7, idx = wg >> 3;
    const int ks = xcd >> 1, mt = (xcd & 1) * 8 + (idx >> 2), nq = idx & 3;
    const int m0 = mt * 128 + wave * 32;
    unsigned avoff[4];
    a_offsets<2>(avoff, m0, SEQ * 2, lane, SEQ - 1);
    const char* A = (const char*)(ks < 2 ? csr : csi) + (ks & 1) * 2048;
    const char* B = ws + WS_B + ((size_t)(ks * 32) * 4 + nq) * 7168;
    f32x4 acc[2][7];
#pragma unroll
    for (int m = 0; m < 2; ++m)
#pragma unroll
      for (int n = 0; n < 7; ++n) acc[m][n] = f32x4{0.f, 0.f, 0.f, 0.f};
    gemm_core<16, 3, 4, 2, 2, true>(A, avoff, B, 4 * 7168, smem, wave, lane, K2ROT, acc);
    TS(2);
    store_rows<2>(acc, smem + wave * 4096, 16384, ws + WS_P + ((size_t)ks * SEQ + m0) * (DM * 2) + nq * 224, DM * 2, lane);
    TSEND(ws, 1);
  }
}

__global__ void __launch_bounds__(256, 1)
k3_final(const __bf16* __restrict__ wo, const char* __restrict__ ws, __bf16* __restrict__ out) {
  extern __shared__ __attribute__((aligned(16))) char smem[];
  const int lane = threadIdx.x & 63;
  const int wave = __builtin_amdgcn_readfirstlane(threadIdx.x >> 6);
  const int bid = blockIdx.x;
  TSDECL TS(0); TS(1); TS(2);
  const int xcd = bid & 7, idx = bid >> 3;
  if (idx < 16) {
    const int rt = xcd * 4 + (idx >> 2), nq = idx & 3;
    const int m0 = rt * 64 + wave * 16;
    const unsigned avoff[2] = {(unsigned)(lane * 16), 0u};
    f32x4 acc[1][7];
#pragma unroll
    for (int n = 0; n < 7; ++n) acc[0][n] = f32x4{0.f, 0.f, 0.f, 0.f};
    gemm_core<8, 8, 16, 1, 3, true>(ws + WS_O + (size_t)(m0 >> 4) * 16384, avoff, (const char*)wo + nq * 224, DM * 2, smem, wave, lane,
                                    K3ROT, acc);
    store_rows<1>(acc, smem + 16 * 7168 + wave * 4096, 0, (char*)out + (size_t)m0 * (2 * DM * 2) + nq * 224, 2 * DM * 2, lane);
  } else {
    const int u0 = (xcd * 14 + idx - 16) * 1024 + threadIdx.x;
    u32x4 t[4][4];
#pragma unroll
    for (int n = 0; n < 4; ++n)
#pragma unroll
      for (int ks = 0; ks < 4; ++ks) t[n][ks] = *(const u32x4*)(ws + WS_P + (size_t)ks * (SEQ * DM * 2) + (size_t)(u0 + n * 256) * 16);
#pragma unroll
    for (int n = 0; n < 4; ++n) {
      u32x4 r;
#pragma unroll
      for (int e = 0; e < 4; ++e) {
        float lo = 0.f, hi = 0.f;
#pragma unroll
        for (int ks = 0; ks < 4; ++ks) {
          lo += __builtin_bit_cast(float, t[n][ks][e] << 16);
          hi += __builtin_bit_cast(float, t[n][ks][e] & 0xffff0000u);
        }
        r[e] = pack2(lo, hi);
      }
      const int u = u0 + n * 256, row = u / 56, ch = u - row * 56;
      *(u32x4*)((char*)out + (size_t)row * (2 * DM * 2) + DM * 2 + ch * 16) = r;
    }
  }
  TSEND(ws, 2);
}

extern "C" void kernel_launch(void* const* d_in, const int* in_sizes, int n_in, void* d_out, int out_size,
                              void* d_ws, size_t ws_size, hipStream_t stream) {
  const __bf16* xa = (const __bf16*)d_in[0];
  const __bf16* wqkv = (const __bf16*)d_in[1];
  const __bf16* wo = (const __bf16*)d_in[2];
  const __bf16* xb = (const __bf16*)d_in[3];
  const __bf16* csr = (const __bf16*)d_in[4];
  const __bf16* csi = (const __bf16*)d_in[5];
  const __bf16* whr = (const __bf16*)d_in[6];
  const __bf16* whi = (const __bf16*)d_in[7];
  char* ws = (char*)d_ws;
  constexpr int L1 = 4 * 24576 + 9 * 7168, L2 = 3 * 16384 + 4 * 7168, L3 = 16 * 7168 + 4 * 4096;
  (void)hipFuncSetAttribute((const void*)k1_proj, hipFuncAttributeMaxDynamicSharedMemorySize, L1);
  (void)hipFuncSetAttribute((const void*)k2_main, hipFuncAttributeMaxDynamicSharedMemorySize, L2);
  (void)hipFuncSetAttribute((const void*)k3_final, hipFuncAttributeMaxDynamicSharedMemorySize, L3);
  k1_proj<<<240, 256, L1, stream>>>(xa, wqkv, xb, whr, whi, ws);
  k2_main<<<512, 256, L2, stream>>>(csr, csi, ws);
  k3_final<<<240, 256, L3, stream>>>(wo, ws, (__bf16*)d_out);
}
```

```cpp
#include <hip/hip_runtime.h>
#include <hip/hip_bf16.h>
#include <stdint.h>
#include <utility>

typedef __bf16 bf16x8 __attribute__((ext_vector_type(8)));
typedef __bf16 bf16x4 __attribute__((ext_vector_type(4)));
typedef __bf16 bf16x2 __attribute__((ext_vector_type(2)));
typedef short s16x4 __attribute__((ext_vector_type(4)));
typedef float f32x4 __attribute__((ext_vector_type(4)));
typedef float f32x16 __attribute__((ext_vector_type(16)));
typedef unsigned u32x4 __attribute__((ext_vector_type(4)));
typedef unsigned u32x2 __attribute__((ext_vector_type(2)));
#define LDSP __attribute__((address_space(3)))

#define TSDECL
#define TS(i)
#define TSEND(wsb, kidx)

#define ATROT 0
#define GPRIO 8
#define K3ROT ((2 * nq + (idx >> 2)) & 7)
#define K3CFG 8, 4, 8
#define PFS 8
#define PFD 1
#define NPF1 0
#define K2CFG 16, 3, 4
#define ABURST(mt) 0
#define WT1 true
#define WT2 true
#define WT3 true
#define K2ROT 0
constexpr int SEQ = 2048, DM = 448, NH = 16, DH = 28;

constexpr size_t WS_Q = 0;
constexpr size_t WS_K = 2u << 20;
constexpr size_t WS_V = 4u << 20;
constexpr size_t WS_B = 6u << 20;
constexpr size_t WS_O = 10u << 20;
constexpr size_t WS_P = 12u << 20;

template <int... Is, class F>
__device__ __forceinline__ void static_for_impl(std::integer_sequence<int, Is...>, F&& f) {
  (f(std::integral_constant<int, Is>{}), ...);
}
template <int N, class F>
__device__ __forceinline__ void static_for(F&& f) {
  static_for_impl(std::make_integer_sequence<int, N>{}, static_cast<F&&>(f));
}

__device__ __forceinline__ unsigned pack2(float a, float b) {
  bf16x2 v; v[0] = (__bf16)a; v[1] = (__bf16)b;
  return __builtin_bit_cast(unsigned, v);
}

template <int NKB, int NSA, int NSB, int MT, int NPF>
struct Sched {
  static constexpr int NS = 2 * NKB;
  static constexpr int VB = 2, VA = 2 * MT;
  static constexpr int PA = NSA >= NKB ? NKB : NSA - 1;
  static constexpr int PB = NSB >= NS ? NS : NSB - 1;
  static constexpr int PM = (PA > (PB + 1) / 2) ? PA : (PB + 1) / 2;
  static constexpr int wait(int s) {
    int pos = 0, endB = 0, endA = 0;
    for (int p = 0; p < PM; ++p) {
      if (p < PA) { pos += VA; if (p == s / 2) endA = pos; }
      if (2 * p < PB) { pos += VB; if (2 * p == s) endB = pos; }
      if (2 * p + 1 < PB) { pos += VB; if (2 * p + 1 == s) endB = pos; }
    }
    for (int it = 0; it < s; ++it) {
      if (it + PB < NS) { pos += VB; if (it + PB == s) endB = pos; }
      if (it / 2 + PA < NKB && !(ABURST(MT) && (it & 1))) { pos += ABURST(MT) ? 2 * MT : MT; if (it / 2 + PA == s / 2) endA = pos; }
      for (int j = 0; j < NPF; ++j) if (it == PFS + j * PFD) pos += 1;
    }
    int e = endB > endA ? endB : endA;
    return pos - e;
  }
};

__device__ __forceinline__ void glds16(unsigned voff, const void* sbase, unsigned lds_dst) {
  unsigned keep;
  asm volatile("s_mov_b32 %0, m0\n\ts_mov_b32 m0, %3\n\ts_nop 0\n\tglobal_load_lds_dwordx4 %1, %2\n\ts_mov_b32 m0, %0"
               : "=&s"(keep) : "v"(voff), "s"(sbase), "s"(lds_dst) : "memory");
}
__device__ __forceinline__ void glds16_48(unsigned voff, const void* sbase, unsigned lds_dst) {
  unsigned keep;
  asm volatile("s_mov_b32 %0, m0\n\ts_mov_b32 m0, %3\n\ts_mov_b32 exec_hi, 0xffff\n\ts_nop 0\n\tglobal_load_lds_dwordx4 %1, %2\n\t"
               "s_mov_b32 exec_hi, -1\n\ts_mov_b32 m0, %0"
               : "=&s"(keep) : "v"(voff), "s"(sbase), "s"(lds_dst) : "memory");
}

__device__ __forceinline__ void glds4(unsigned voff, const void* sbase, unsigned lds_dst) {
  unsigned keep;
  asm volatile("s_mov_b32 %0, m0\n\ts_mov_b32 m0, %3\n\ts_nop 0\n\tglobal_load_lds_dword %1, %2\n\ts_mov_b32 m0, %0"
               : "=&s"(keep) : "v"(voff), "s"(sbase), "s"(lds_dst) : "memory");
}

template <bool WT>
__device__ __forceinline__ void st16(void* p, u32x4 v) {
  if constexpr (WT) asm volatile("global_store_dwordx4 %0, %1, off sc1\n\ts_nop 1" ::"v"(p), "v"(v) : "memory");
  else *(u32x4*)p = v;
}

template <int OFF>
__device__ __forceinline__ void gload16(bf16x8& dst, unsigned voff, const void* sbase) {
  asm volatile("global_load_dwordx4 %0, %1, %2 offset:%3" : "=v"(dst) : "v"(voff), "s"(sbase), "i"(OFF) : "memory");
}
__device__ __forceinline__ void tie2(bf16x8& x, bf16x8& y) { asm volatile("" : "+v"(x), "+v"(y)); }

template <int NKB, int NSA, int NSB, int MT, int BKIND, bool SWAP, int NPF = 0>
__device__ __forceinline__ void gemm_core(const char* Abase, const unsigned (&avoff)[2 * MT], const char* Bbase, int ldb2,
                                          char* smem, int wave, int lane, int rot, f32x4 (&acc)[MT][7],
                                          const char* pfbase = nullptr, int pfline = 0) {
  using SC = Sched<NKB, NSA, NSB, MT, NPF>;
  constexpr int NS = SC::NS, PA = SC::PA, PB = SC::PB;
  constexpr int ASLOT = MT * 8192;
  constexpr bool BTR = BKIND == 0 || BKIND == 3, ADIR = BKIND == 3;
  constexpr int BOFF = ADIR ? 0 : NSA * ASLOT;
  const unsigned lds0 = (unsigned)(uintptr_t)(LDSP char*)smem;
  unsigned bvoff[2];
  static_for<2>([&](auto P) {
    constexpr int p = P;
    int L = wave * 1792 + p * 1024 + lane * 16;
    if (p == 1 && lane >= 48) L = wave * 1792;
    if constexpr (BTR) {
      int pos = L / 224, ch = (L - pos * 224) >> 4;
      int k = (pos & 3) + 8 * ((pos >> 2) & 1) + 4 * ((pos >> 3) & 1) + (pos & 16);
      if constexpr (BKIND == 3) k = k < 28 ? k : 27;
      bvoff[p] = k * ldb2 + ch * 16;
    } else if constexpr (BKIND == 1) {
      int n = L >> 6, chp = (L >> 4) & 3;
      bvoff[p] = n * ldb2 + 16 * (chp ^ ((-(n >> 2)) & 3));
    } else {
      bvoff[p] = L;
    }
  });
  const int bstep = (BKIND == 0) ? 32 * ldb2 : (BKIND == 1 ? 64 : (BKIND == 2 ? ldb2 : 28 * ldb2));
  bf16x8 fd[ADIR ? NS : 1][MT];
  auto issueB1 = [&](auto S, auto P) {
    constexpr int s = S;
    int se = s + 2 * rot;
    se = se >= NS ? se - NS : se;
    const char* src = Bbase + (ptrdiff_t)se * bstep;
    const unsigned d = lds0 + BOFF + (s % NSB) * 7168 + wave * 1792;
    if constexpr (P == 0) glds16(bvoff[0], src, d);
    else glds16_48(bvoff[1], src, d + 1024);
  };
  auto issueA1 = [&](auto KB, auto P) {
    constexpr int kb = KB;
    int ke = kb + rot;
    ke = ke >= NKB ? ke - NKB : ke;
    if constexpr (ADIR) {
      gload16<(P & 1) * 1024>(fd[2 * kb + (P & 1)][P >> 1], avoff[P & ~1], Abase + ke * 2048);
    } else {
      const char* src = Abase + ke * 128;
      const unsigned d = lds0 + (kb % NSA) * ASLOT + wave * (MT * 2048);
      glds16(avoff[P], src, d + P * 1024);
    }
  };
  auto issueB = [&](auto S) { static_for<2>([&](auto P) { issueB1(S, P); }); };
  auto issueA = [&](auto KB) { static_for<2 * MT>([&](auto P) { issueA1(KB, P); }); };
  auto stepload = [&](auto S, auto J) {
    constexpr int s = S, j = J;
    if constexpr (j < 2) {
      if constexpr (s + PB < NS) issueB1(std::integral_constant<int, s + PB>{}, J);
    } else {
      if constexpr (s / 2 + PA < NKB) {
        if constexpr (!ABURST(MT)) {
          issueA1(std::integral_constant<int, s / 2 + PA>{}, std::integral_constant<int, (s & 1) * MT + j - 2>{});
        } else if constexpr ((s & 1) == 0) {
          issueA1(std::integral_constant<int, s / 2 + PA>{}, std::integral_constant<int, 2 * (j - 2)>{});
          issueA1(std::integral_constant<int, s / 2 + PA>{}, std::integral_constant<int, 2 * (j - 2) + 1>{});
        }
      }
    }
  };
  static_for<SC::PM>([&](auto P) {
    constexpr int p = P;
    if constexpr (p < PA) issueA(P);
    if constexpr (2 * p < PB) issueB(std::integral_constant<int, 2 * p>{});
    if constexpr (2 * p + 1 < PB) issueB(std::integral_constant<int, 2 * p + 1>{});
  });
  const int c = lane & 15, g = lane >> 4;
  int rb;
  if constexpr (BTR) rb = ((c >> 2) + 4 * (g & 1) + 16 * (g >> 1)) * 224 + 8 * (c & 3);
  else if constexpr (BKIND == 1) rb = c * 64 + 16 * (g ^ ((-(c >> 2)) & 3));
  else rb = lane * 16;
  const int ra0 = wave * (MT * 2048) + c * 128 + 16 * (g ^ (c >> 1));
  const int ra1 = ra0 ^ 64;

  bf16x8 fa[2][MT], fb[2][7];
  auto sync = [&](auto S) {
    constexpr int s = S;
    asm volatile("s_waitcnt vmcnt(%0) lgkmcnt(0)" ::"i"(SC::wait(s)) : "memory");
    __builtin_amdgcn_s_barrier();
    if constexpr (BKIND == 2 && s == 0 && GPRIO > 0) __builtin_amdgcn_s_setprio(1);
    if constexpr (BKIND == 2 && s == GPRIO && GPRIO > 0) __builtin_amdgcn_s_setprio(0);
    if constexpr (ADIR && (s & 1) == 0) static_for<MT>([&](auto M) { tie2(fd[s][M], fd[s + 1][M]); });
    __builtin_amdgcn_sched_barrier(0);
  };
  constexpr int NRA = ADIR ? 0 : MT;
  constexpr int NSIDE = NRA + 7 + 2 + MT;
  auto side = [&](auto S, auto K) {
    constexpr int s = S, k = K;
    if constexpr (k < NRA) {
      const char* sa = smem + ((s / 2) % NSA) * ASLOT + ((s & 1) ? ra1 : ra0);
      fa[s & 1][k] = *(const bf16x8*)(sa + k * 2048);
    } else if constexpr (k < NRA + 7) {
      constexpr int nt = k - NRA;
      const char* sb = smem + BOFF + (s % NSB) * 7168 + rb;
      if constexpr (BTR) {
        s16x4 lo = __builtin_amdgcn_ds_read_tr16_b64_v4i16((LDSP s16x4*)(sb + 32 * nt));
        s16x4 hi = __builtin_amdgcn_ds_read_tr16_b64_v4i16((LDSP s16x4*)(sb + 8 * 224 + 32 * nt));
        typedef short s16x8 __attribute__((ext_vector_type(8)));
        s16x8 t = __builtin_shufflevector(lo, hi, 0, 1, 2, 3, 4, 5, 6, 7);
        fb[s & 1][nt] = __builtin_bit_cast(bf16x8, t);
      } else {
        fb[s & 1][nt] = *(const bf16x8*)(sb + nt * 1024);
      }
    } else {
      stepload(S, std::integral_constant<int, k - NRA - 7>{});
      if constexpr (k == NSIDE - 1) {
        static_for<NPF>([&](auto J) {
          if constexpr (s == PFS + J * PFD) {
            int L = pfline + J * 64 + (J == 2 && lane >= 9 ? 0 : lane);
            L = L < 16384 ? L : 16383;
            glds4((L >> 4) * (SEQ * 2) + (L & 15) * 128, pfbase, lds0 + BOFF + NSB * 7168 + wave * 256);
          }
        });
      }
    }
    __builtin_amdgcn_sched_barrier(0);
  };
  sync(std::integral_constant<int, 0>{});
  static_for<NSIDE>([&](auto K) { side(std::integral_constant<int, 0>{}, K); });
  static_for<NS>([&](auto S) {
    constexpr int s = S;
    if constexpr (s + 1 < NS) sync(std::integral_constant<int, s + 1>{});
    static_for<7>([&](auto N) {
      constexpr int nt = N;
      static_for<MT>([&](auto M) {
        constexpr int m = M;
        const bf16x8 av = ADIR ? fd[ADIR ? s : 0][m] : fa[s & 1][m];
        if constexpr (SWAP)
          acc[m][nt] = __builtin_amdgcn_mfma_f32_16x16x32_bf16(fb[s & 1][nt], av, acc[m][nt], 0, 0, 0);
        else
          acc[m][nt] = __builtin_amdgcn_mfma_f32_16x16x32_bf16(av, fb[s & 1][nt], acc[m][nt], 0, 0, 0);
        __builtin_amdgcn_sched_barrier(0);
        if constexpr (s + 1 < NS) {
          static_for<NSIDE>([&](auto K) {
            if constexpr ((K * 7 * MT) / NSIDE == nt * MT + m) side(std::integral_constant<int, s + 1>{}, K);
          });
        }
      });
    });
  });
}

template <int MT>
__device__ __forceinline__ void a_offsets(unsigned (&avoff)[2 * MT], int m0, int lda2, int lane, int maxrow) {
#pragma unroll
  for (int p = 0; p < 2 * MT; ++p) {
    const int r = 8 * p + (lane >> 3);
    int row = m0 + r;
    row = row < maxrow ? row : maxrow;
    avoff[p] = row * lda2 + 16 * ((lane & 7) ^ ((r >> 1) & 7));
  }
}

template <int MT>
__device__ __forceinline__ void store_rows(const f32x4 (&acc)[MT][7], char* tb, int tstride, char* dst, int ld2, int lane) {
  const int c = lane & 15, g = lane >> 4;
#pragma unroll
  for (int m = 0; m < MT; ++m)
#pragma unroll
    for (int n = 0; n < 7; ++n)
      *(u32x2*)(tb + m * tstride + c * 240 + n * 32 + g * 8) = u32x2{pack2(acc[m][n][0], acc[m][n][1]), pack2(acc[m][n][2], acc[m][n][3])};
#pragma unroll
  for (int m = 0; m < MT; ++m)
#pragma unroll
    for (int i = 0; i < 4; ++i) {
      const int u = i * 64 + lane;
      const int row = (u * 4682) >> 16, ch = u - row * 14;
      if (i < 3 || lane < 32) {
        const u32x4 v = *(const u32x4*)(tb + m * tstride + row * 240 + ch * 16);
        st16<MT == 2 ? WT2 : WT3>(dst + (size_t)(m * 16 + row) * ld2 + ch * 16, v);
      }
    }
}

constexpr int K1_MT = 3;
#define K1CFG 7, 3, 4
__global__ void __launch_bounds__(256, 1)
k1_proj(const __bf16* __restrict__ xa, const __bf16* __restrict__ wqkv, const __bf16* __restrict__ xb,
        const __bf16* __restrict__ whr, const __bf16* __restrict__ whi, const __bf16* __restrict__ csr,
        const __bf16* __restrict__ csi, char* __restrict__ ws) {
  extern __shared__ __attribute__((aligned(16))) char smem[];
  const int lane = threadIdx.x & 63;
  const int wave = __builtin_amdgcn_readfirstlane(threadIdx.x >> 6);
  const int bid = blockIdx.x;
  TSDECL TS(0); TS(1);
  const int xcd = bid & 7, idx = bid >> 3;
  const int rt = idx / 10, ci = idx - 10 * rt;
  const int ct = (xcd & 1) * 10 + ci;
  const int lim = (xcd >> 1) * 512 + 512;
  const int m0 = (xcd >> 1) * 512 + rt * 192 + wave * 48;
  const int rot = (ci + 3 * rt) % 7;
  const int c = lane & 15, g = lane >> 4;
  unsigned avoff[2 * K1_MT];
  a_offsets<K1_MT>(avoff, m0, DM * 2, lane, lim - 1);
  f32x4 acc[K1_MT][7];
#pragma unroll
  for (int m = 0; m < K1_MT; ++m)
#pragma unroll
    for (int n = 0; n < 7; ++n) acc[m][n] = f32x4{0.f, 0.f, 0.f, 0.f};

  const char* pfb = (const char*)(xcd < 4 ? csr : csi) + ((xcd >> 1) & 1) * 2048 + (size_t)(xcd & 1) * 1024 * (SEQ * 2);
  const int pfl = (idx * 4 + wave) * 137;
  char* tb = smem + wave * 6144;
  if (ct < 8) {
    gemm_core<K1CFG, K1_MT, 0, true, NPF1>((const char*)xa, avoff, (const char*)wqkv + ct * 224, 3 * DM * 2, smem, wave, lane, rot, acc, pfb, pfl);
    TS(2);
    const bool isq = ct < 4;
    const float sc = isq ? 0.18898223650461362f * 1.4426950408889634f : 1.0f;
    char* dst = ws + (isq ? WS_Q : WS_K);
    const unsigned padlo = isq ? 0u : 0x3f803f80u, padhi = isq ? 0u : 0x00003f80u;
#pragma unroll
    for (int m = 0; m < K1_MT; ++m) {
      char* st = tb + m * 24576;
#pragma unroll
      for (int nt = 0; nt < 7; ++nt) {
        const int n4 = 4 * nt + g;
        const int hh = (n4 * 37) >> 8;
        const int q4 = n4 - 7 * hh;
        char* p = st + (hh * 4 + (q4 >> 1)) * 256 + c * 16 + (q4 & 1) * 8;
        *(u32x2*)p = u32x2{pack2(acc[m][nt][0] * sc, acc[m][nt][1] * sc), pack2(acc[m][nt][2] * sc, acc[m][nt][3] * sc)};
        if (q4 == 6) *(u32x2*)(p + 8) = u32x2{padlo, padhi};
      }
    }
#pragma unroll
    for (int m = 0; m < K1_MT; ++m) {
      const int t0 = m0 + 16 * m;
      if (t0 < lim) {
#pragma unroll
        for (int hh = 0; hh < 4; ++hh) {
          const u32x4 v = *(const u32x4*)(tb + m * 24576 + hh * 1024 + lane * 16);
          st16<WT1>(dst + ((4 * (ct & 3) + hh) * 64 + (t0 >> 5)) * 2048 + g * 512 + ((t0 & 16) + c) * 16, v);
        }
      }
    }
  } else if (ct < 12) {
    gemm_core<K1CFG, K1_MT, 0, false, NPF1>((const char*)xa, avoff, (const char*)wqkv + ct * 224, 3 * DM * 2, smem, wave, lane, rot, acc, pfb, pfl);
    TS(2);
    char* dst = ws + WS_V;
#pragma unroll
    for (int m = 0; m < K1_MT; ++m) {
      char* st = tb + m * 24576;
#pragma unroll
      for (int nt = 0; nt < 7; ++nt) {
        const int nl = 16 * nt + c;
        const int hh = (nl * 147) >> 12;
        const int d = nl - 28 * hh;
        char* p = st + ((hh * 2 + (g & 1)) * 32 + d) * 16 + (g >> 1) * 8;
        *(u32x2*)p = u32x2{pack2(acc[m][nt][0], acc[m][nt][1]), pack2(acc[m][nt][2], acc[m][nt][3])};
      }
      if (lane < 32) {
        const unsigned o1 = (lane & 3) == 0 ? 0x3f803f80u : 0u;
        *(u32x4*)(st + ((lane >> 2) * 32 + 28 + (lane & 3)) * 16) = u32x4{o1, o1, o1, o1};
      }
    }
#pragma unroll
    for (int m = 0; m < K1_MT; ++m) {
      const int t0 = m0 + 16 * m;
      if (t0 < lim) {
#pragma unroll
        for (int hh = 0; hh < 4; ++hh) {
          const u32x4 v = *(const u32x4*)(tb + m * 24576 + hh * 1024 + lane * 16);
          st16<WT1>(dst + ((4 * (ct & 3) + hh) * 128 + (t0 >> 4)) * 1024 + lane * 16, v);
        }
      }
    }
  } else {
    const bool isb = ct >= 16;
    const int nq = ct & 3;
    const char* W = (const char*)(isb ? whi : whr) + (size_t)nq * 112 * (DM * 2);
    gemm_core<K1CFG, K1_MT, 1, false, NPF1>((const char*)xb, avoff, W, DM * 2, smem, wave, lane, rot, acc, pfb, pfl);
    TS(2);
    const float sg = isb ? -1.f : 1.f;
    char* dst = ws + WS_B;
#pragma unroll
    for (int m = 0; m < K1_MT; ++m) {
      char* st = tb + m * 24576;
#pragma unroll
      for (int nt = 0; nt < 7; ++nt)
        *(u32x2*)(st + nt * 512 + (g >> 1) * 256 + c * 16 + (g & 1) * 8) =
            u32x2{pack2(acc[m][nt][0] * sg, acc[m][nt][1] * sg), pack2(acc[m][nt][2] * sg, acc[m][nt][3] * sg)};
    }
#pragma unroll
    for (int m = 0; m < K1_MT; ++m) {
      const int t0 = m0 + 16 * m;
      if (t0 < lim) {
        const int kk = t0 + (isb ? SEQ : 0);
        char* gp = dst + ((kk >> 5) * 4 + nq) * 7168 + (kk & 16) * 32 + (lane >> 5) * 1024 + (lane & 31) * 16;
#pragma unroll
        for (int i = 0; i < 4; ++i) {
          if (i < 3 || lane < 32) {
            const u32x4 v = *(const u32x4*)(tb + m * 24576 + i * 1024 + lane * 16);
            st16<WT1>(gp + i * 2048, v);
          }
        }
      }
    }
  }
  TSEND(ws, 0);
}

__device__ __forceinline__ float xhalf_max(float v) {
  auto r = __builtin_amdgcn_permlane32_swap(__builtin_bit_cast(unsigned, v), __builtin_bit_cast(unsigned, v), false, false);
  return fmaxf(__builtin_bit_cast(float, (unsigned)r[0]), __builtin_bit_cast(float, (unsigned)r[1]));
}
__device__ __forceinline__ float tile_max(const f32x16& s) {
  float m = fmaxf(fmaxf(s[0], s[1]), s[2]);
#pragma unroll
  for (int i = 3; i < 15; i += 2) m = fmaxf(fmaxf(m, s[i]), s[i + 1]);
  m = fmaxf(m, s[15]);
  return xhalf_max(m);
}
__device__ __forceinline__ void exp_pack(const f32x16& s, bf16x8& p0, bf16x8& p1) {
#pragma unroll
  for (int j = 0; j < 8; ++j) {
    p0[j] = (__bf16)__builtin_amdgcn_exp2f(s[j]);
    p1[j] = (__bf16)__builtin_amdgcn_exp2f(s[8 + j]);
  }
}

__device__ __forceinline__ void attn_role(const char* __restrict__ ws, char* __restrict__ wso, char* smem, int wg, int wave, int lane) {
  TSDECL TS(0);
  const int xcd = wg & 7, idx = wg >> 3;
  const int head = xcd * 2 + (idx >> 4), qb = idx & 15;
  const bf16x8* Kf = (const bf16x8*)(ws + WS_K) + (size_t)(head * 64 + wave * 16) * 128 + lane;
  const bf16x8* Vf = (const bf16x8*)(ws + WS_V) + (size_t)(head * 64 + wave * 16) * 128 + lane;
  const f32x16 zero16 = {0.f, 0.f, 0.f, 0.f, 0.f, 0.f, 0.f, 0.f, 0.f, 0.f, 0.f, 0.f, 0.f, 0.f, 0.f, 0.f};

  const char* Kb = (const char*)(ws + WS_K) + (size_t)(head * 64 + wave * 16) * 2048;
  const unsigned l16 = lane * 16;
  bf16x8 kf[3][2], vf[3][2];
  bf16x8 qf[4][2];
  auto loadkv = [&](auto T) {
    constexpr int t = T;
    const char* src = Kb + ((t + ATROT) & 15) * 2048;
    gload16<0>(kf[t % 3][0], l16, src);
    gload16<1024>(kf[t % 3][1], l16, src);
    gload16<0>(vf[t % 3][0], l16, src + (WS_V - WS_K));
    gload16<1024>(vf[t % 3][1], l16, src + (WS_V - WS_K));
  };
  loadkv(std::integral_constant<int, 0>{});
  {
    const char* Qb = (const char*)(ws + WS_Q) + (size_t)(head * 64 + qb * 4) * 2048;
    static_for<4>([&](auto QT) {
      gload16<0>(qf[QT][0], l16, Qb + QT * 2048);
      gload16<1024>(qf[QT][1], l16, Qb + QT * 2048);
    });
  }
  loadkv(std::integral_constant<int, 1>{});
  asm volatile("s_waitcnt vmcnt(4)" ::: "memory");
  tie2(kf[0][0], kf[0][1]); tie2(vf[0][0], vf[0][1]);
#pragma unroll
  for (int qt = 0; qt < 4; ++qt) tie2(qf[qt][0], qf[qt][1]);
  const bool hi = lane >= 32;
  float mref[4];
#pragma unroll
  for (int qt = 0; qt < 4; ++qt) {
    f32x16 s = __builtin_amdgcn_mfma_f32_32x32x16_bf16(kf[0][0], qf[qt][0], zero16, 0, 0, 0);
    s = __builtin_amdgcn_mfma_f32_32x32x16_bf16(kf[0][1], qf[qt][1], s, 0, 0, 0);
    const float nm = -(tile_max(s) + 64.f);
    const __bf16 b0 = (__bf16)nm;
    const float r1 = nm - (float)b0;
    const __bf16 b1 = (__bf16)r1;
    const float r2 = r1 - (float)b1;
    const __bf16 b2 = (__bf16)r2;
    mref[qt] = -(((float)b0 + (float)b1) + (float)b2);
    if (hi) { qf[qt][1][4] = b0; qf[qt][1][5] = b1; qf[qt][1][6] = b2; }
  }
  TS(1);
  f32x16 o[4];
#pragma unroll
  for (int qt = 0; qt < 4; ++qt) o[qt] = zero16;

  {
#define SB() __builtin_amdgcn_sched_barrier(0)
    f32x16 sc[2];
    u32x4 pa, pb;
    float ea[4], eb[4];
    sc[0] = __builtin_amdgcn_mfma_f32_32x32x16_bf16(kf[0][0], qf[0][0], zero16, 0, 0, 0);
    sc[0] = __builtin_amdgcn_mfma_f32_32x32x16_bf16(kf[0][1], qf[0][1], sc[0], 0, 0, 0);
    SB();
    static_for<64>([&](auto I) {
      constexpr int i = I, kt = i >> 2, qt = i & 3;
      constexpr int ni = i + 1, nkt = ni >> 2, nqt = ni & 3;
      constexpr int pi = i - 1, pkt = pi >> 2, pqt = pi & 3;
      const f32x16& cur = sc[i & 1];
      if constexpr (qt == 3 && ni < 64) {
        asm volatile("s_waitcnt vmcnt(%0)" ::"i"(kt + 2 < 16 ? 4 : 0) : "memory");
        tie2(kf[nkt % 3][0], kf[nkt % 3][1]); tie2(vf[nkt % 3][0], vf[nkt % 3][1]);
      }
      if constexpr (ni < 64) sc[ni & 1] = __builtin_amdgcn_mfma_f32_32x32x16_bf16(kf[nkt % 3][0], qf[nqt][0], zero16, 0, 0, 0);
      SB();
      if constexpr (i > 0) { pb[2] = pack2(eb[0], eb[1]); pb[3] = pack2(eb[2], eb[3]); }
      SB();
#pragma unroll
      for (int j = 0; j < 4; ++j) ea[j] = __builtin_amdgcn_exp2f(cur[j]);
      SB();
      if constexpr (i > 0) o[pqt] = __builtin_amdgcn_mfma_f32_32x32x16_bf16(vf[pkt % 3][1], __builtin_bit_cast(bf16x8, pb), o[pqt], 0, 0, 0);
      SB();
      pa[0] = pack2(ea[0], ea[1]); pa[1] = pack2(ea[2], ea[3]);
      SB();
#pragma unroll
      for (int j = 0; j < 4; ++j) eb[j] = __builtin_amdgcn_exp2f(cur[4 + j]);
      if constexpr (qt == 0 && kt + 2 < 16) loadkv(std::integral_constant<int, kt + 2>{});
      SB();
      if constexpr (ni < 64) sc[ni & 1] = __builtin_amdgcn_mfma_f32_32x32x16_bf16(kf[nkt % 3][1], qf[nqt][1], sc[ni & 1], 0, 0, 0);
      SB();
      pa[2] = pack2(eb[0], eb[1]); pa[3] = pack2(eb[2], eb[3]);
      SB();
#pragma unroll
      for (int j = 0; j < 4; ++j) ea[j] = __builtin_amdgcn_exp2f(cur[8 + j]);
      SB();
      o[qt] = __builtin_amdgcn_mfma_f32_32x32x16_bf16(vf[kt % 3][0], __builtin_bit_cast(bf16x8, pa), o[qt], 0, 0, 0);
      SB();
      pb[0] = pack2(ea[0], ea[1]); pb[1] = pack2(ea[2], ea[3]);
      SB();
#pragma unroll
      for (int j = 0; j < 4; ++j) eb[j] = __builtin_amdgcn_exp2f(cur[12 + j]);
      SB();
    });
    pb[2] = pack2(eb[0], eb[1]); pb[3] = pack2(eb[2], eb[3]);
    o[3] = __builtin_amdgcn_mfma_f32_32x32x16_bf16(vf[15 % 3][1], __builtin_bit_cast(bf16x8, pb), o[3], 0, 0, 0);
  }

  TS(2);
  bool bad = false;
#pragma unroll
  for (int qt = 0; qt < 4; ++qt) {
    const float l = o[qt][12];
    bad |= hi && !(l >= 0x1p-80f && l <= 0x1p64f);
    float am = fabsf(o[qt][0]);
#pragma unroll
    for (int j = 1; j < 16; ++j) am += fabsf(o[qt][j]);
    bad |= !(am <= 0x1p120f);
  }
  if (__any(bad)) {
#pragma unroll
    for (int qt = 0; qt < 4; ++qt) {
      if (hi) { qf[qt][1][4] = (__bf16)0.f; qf[qt][1][5] = (__bf16)0.f; qf[qt][1][6] = (__bf16)0.f; }
      o[qt] = zero16;
      mref[qt] = -INFINITY;
    }
#pragma unroll 1
    for (int kt = 0; kt < 16; ++kt) {
      const bf16x8 k0 = Kf[kt * 128], k1 = Kf[kt * 128 + 64], v0 = Vf[kt * 128], v1 = Vf[kt * 128 + 64];
#pragma unroll
      for (int qt = 0; qt < 4; ++qt) {
        f32x16 s = __builtin_amdgcn_mfma_f32_32x32x16_bf16(k0, qf[qt][0], zero16, 0, 0, 0);
        s = __builtin_amdgcn_mfma_f32_32x32x16_bf16(k1, qf[qt][1], s, 0, 0, 0);
        const float mn = fmaxf(mref[qt], tile_max(s));
        const float al = __builtin_amdgcn_exp2f(mref[qt] - mn);
        mref[qt] = mn;
#pragma unroll
        for (int j = 0; j < 16; ++j) { s[j] -= mn; o[qt][j] *= al; }
        bf16x8 p0, p1;
        exp_pack(s, p0, p1);
        o[qt] = __builtin_amdgcn_mfma_f32_32x32x16_bf16(v0, p0, o[qt], 0, 0, 0);
        o[qt] = __builtin_amdgcn_mfma_f32_32x32x16_bf16(v1, p1, o[qt], 0, 0, 0);
      }
    }
  }

  f32x4* part = (f32x4*)smem;
  float* mbuf = (float*)(smem + 65536);
#pragma unroll
  for (int qt = 0; qt < 4; ++qt) {
    mbuf[(qt * 4 + wave) * 64 + lane] = hi ? mref[qt] + __builtin_amdgcn_logf(o[qt][12]) : mref[qt];
    if (qt != wave) {
#pragma unroll
      for (int gq = 0; gq < 4; ++gq)
        part[((qt * 4 + wave) * 4 + gq) * 64 + lane] = f32x4{o[qt][4 * gq], o[qt][4 * gq + 1], o[qt][4 * gq + 2], o[qt][4 * gq + 3]};
    }
  }
  f32x16 mine = wave == 0 ? o[0] : (wave == 1 ? o[1] : (wave == 2 ? o[2] : o[3]));
  __syncthreads();
  float mw[4];
#pragma unroll
  for (int w = 0; w < 4; ++w) mw[w] = mbuf[(wave * 4 + w) * 64 + 32 + (lane & 31)];
  const float mm = fmaxf(fmaxf(mw[0], mw[1]), fmaxf(mw[2], mw[3]));
#pragma unroll
  for (int w = 0; w < 4; ++w) mw[w] = mbuf[(wave * 4 + w) * 64 + (lane & 31)];
  float r[16];
#pragma unroll
  for (int j = 0; j < 16; ++j) r[j] = 0.f;
#pragma unroll
  for (int w = 0; w < 4; ++w) {
    const float f = __builtin_amdgcn_exp2f(mw[w] - mm);
#pragma unroll
    for (int gq = 0; gq < 4; ++gq) {
      f32x4 t;
      if (w == wave) t = f32x4{mine[4 * gq], mine[4 * gq + 1], mine[4 * gq + 2], mine[4 * gq + 3]};
      else t = part[((wave * 4 + w) * 4 + gq) * 64 + lane];
#pragma unroll
      for (int e = 0; e < 4; ++e) r[4 * gq + e] += f * t[e];
    }
  }
  const float l = __shfl(r[12], (lane & 31) + 32);
  const float inv = 1.0f / l;
  unsigned w[8];
#pragma unroll
  for (int gq = 0; gq < 4; ++gq) {
    w[2 * gq] = pack2(r[4 * gq] * inv, r[4 * gq + 1] * inv);
    w[2 * gq + 1] = pack2(r[4 * gq + 2] * inv, r[4 * gq + 3] * inv);
  }
  if (hi) { w[6] = 0u; w[7] = 0u; }
  char* op = wso + WS_O + ((size_t)(qb * 8 + wave * 2 + ((lane >> 4) & 1)) * 16 + head) * 1024 + (hi ? 256 : 0) + (lane & 15) * 16;
#pragma unroll
  for (int pr = 0; pr < 2; ++pr) {
    auto s0 = __builtin_amdgcn_permlane32_swap(w[4 * pr], w[4 * pr + 2], false, false);
    auto s1 = __builtin_amdgcn_permlane32_swap(w[4 * pr + 1], w[4 * pr + 3], false, false);
    st16<WT2>(op + pr * 512, u32x4{(unsigned)s0[0], (unsigned)s1[0], (unsigned)s0[1], (unsigned)s1[1]});
  }
  TSEND(wso, 1);
}

__global__ void __launch_bounds__(256, 2)
k2_main(const __bf16* __restrict__ csr, const __bf16* __restrict__ csi, char* __restrict__ ws) {
  extern __shared__ __attribute__((aligned(16))) char smem[];
  const int lane = threadIdx.x & 63;
  const int wave = __builtin_amdgcn_readfirstlane(threadIdx.x >> 6);
  const int bid = blockIdx.x;
  if (bid < 256) {
    attn_role(ws, ws, smem, bid, wave, lane);
  } else {
    TSDECL TS(0); TS(1);
    const int wg = bid - 256;
    const int xcd = wg & 7, idx = wg >> 3;
    const int ks = xcd >> 1, mt = (xcd & 1) * 8 + (idx >> 2), nq = idx & 3;
    const int m0 = mt * 128 + wave * 32;
    unsigned avoff[4];
    a_offsets<2>(avoff, m0, SEQ * 2, lane, SEQ - 1);
    const char* A = (const char*)(ks < 2 ? csr : csi) + (ks & 1) * 2048;
    const char* B = ws + WS_B + ((size_t)(ks * 32) * 4 + nq) * 7168;
    f32x4 acc[2][7];
#pragma unroll
    for (int m = 0; m < 2; ++m)
#pragma unroll
      for (int n = 0; n < 7; ++n) acc[m][n] = f32x4{0.f, 0.f, 0.f, 0.f};
    gemm_core<K2CFG, 2, 2, true>(A, avoff, B, 4 * 7168, smem, wave, lane, K2ROT, acc);
    TS(2);
    store_rows<2>(acc, smem + wave * 4096, 16384, ws + WS_P + ((size_t)ks * SEQ + m0) * (DM * 2) + nq * 224, DM * 2, lane);
    TSEND(ws, 1);
  }
}

__global__ void __launch_bounds__(256, 1)
k3_final(const __bf16* __restrict__ wo, const char* __restrict__ ws, __bf16* __restrict__ out) {
  extern __shared__ __attribute__((aligned(16))) char smem[];
  const int lane = threadIdx.x & 63;
  const int wave = __builtin_amdgcn_readfirstlane(threadIdx.x >> 6);
  const int bid = blockIdx.x;
  TSDECL TS(0); TS(1); TS(2);
  const int xcd = bid & 7, idx = bid >> 3;
  if (idx < 16) {
    const int rt = xcd * 4 + (idx >> 2), nq = idx & 3;
    const int m0 = rt * 64 + wave * 16;
    const unsigned avoff[2] = {(unsigned)(lane * 16), 0u};
    f32x4 acc[1][7];
#pragma unroll
    for (int n = 0; n < 7; ++n) acc[0][n] = f32x4{0.f, 0.f, 0.f, 0.f};
    gemm_core<K3CFG, 1, 3, true>(ws + WS_O + (size_t)(m0 >> 4) * 16384, avoff, (const char*)wo + nq * 224, DM * 2, smem, wave, lane,
                                    K3ROT, acc);
    store_rows<1>(acc, smem + 16 * 7168 + wave * 4096, 0, (char*)out + (size_t)m0 * (2 * DM * 2) + nq * 224, 2 * DM * 2, lane);
  } else {
    const int u0 = (xcd * 14 + idx - 16) * 1024 + threadIdx.x;
    u32x4 t[4][4];
#pragma unroll
    for (int n = 0; n < 4; ++n)
#pragma unroll
      for (int ks = 0; ks < 4; ++ks) t[n][ks] = *(const u32x4*)(ws + WS_P + (size_t)ks * (SEQ * DM * 2) + (size_t)(u0 + n * 256) * 16);
#pragma unroll
    for (int n = 0; n < 4; ++n) {
      u32x4 r;
#pragma unroll
      for (int e = 0; e < 4; ++e) {
        float lo = 0.f, hi = 0.f;
#pragma unroll
        for (int ks = 0; ks < 4; ++ks) {
          lo += __builtin_bit_cast(float, t[n][ks][e] << 16);
          hi += __builtin_bit_cast(float, t[n][ks][e] & 0xffff0000u);
        }
        r[e] = pack2(lo, hi);
      }
      const int u = u0 + n * 256, row = u / 56, ch = u - row * 56;
      st16<WT3>((char*)out + (size_t)row * (2 * DM * 2) + DM * 2 + ch * 16, r);
    }
  }
  TSEND(ws, 2);
}

extern "C" void kernel_launch(void* const* d_in, const int* in_sizes, int n_in, void* d_out, int out_size,
                              void* d_ws, size_t ws_size, hipStream_t stream) {
  const __bf16* xa = (const __bf16*)d_in[0];
  const __bf16* wqkv = (const __bf16*)d_in[1];
  const __bf16* wo = (const __bf16*)d_in[2];
  const __bf16* xb = (const __bf16*)d_in[3];
  const __bf16* csr = (const __bf16*)d_in[4];
  const __bf16* csi = (const __bf16*)d_in[5];
  const __bf16* whr = (const __bf16*)d_in[6];
  const __bf16* whi = (const __bf16*)d_in[7];
  char* ws = (char*)d_ws;
  constexpr int L1 = 3 * 24576 + 4 * 7168 + 1024, L2 = 81920, L3 = 16 * 7168 + 4 * 4096;
  (void)hipFuncSetAttribute((const void*)k1_proj, hipFuncAttributeMaxDynamicSharedMemorySize, L1);
  (void)hipFuncSetAttribute((const void*)k2_main, hipFuncAttributeMaxDynamicSharedMemorySize, L2);
  (void)hipFuncSetAttribute((const void*)k3_final, hipFuncAttributeMaxDynamicSharedMemorySize, L3);
  k1_proj<<<240, 256, L1, stream>>>(xa, wqkv, xb, whr, whi, csr, csi, ws);
  k2_main<<<512, 256, L2, stream>>>(csr, csi, ws);
  k3_final<<<240, 256, L3, stream>>>(wo, ws, (__bf16*)d_out);
}
```

```cpp
#include <hip/hip_runtime.h>
#include <hip/hip_bf16.h>
#include <stdint.h>
#include <utility>

typedef __bf16 bf16x8 __attribute__((ext_vector_type(8)));
typedef __bf16 bf16x4 __attribute__((ext_vector_type(4)));
typedef __bf16 bf16x2 __attribute__((ext_vector_type(2)));
typedef short s16x4 __attribute__((ext_vector_type(4)));
typedef float f32x4 __attribute__((ext_vector_type(4)));
typedef float f32x16 __attribute__((ext_vector_type(16)));
typedef unsigned u32x4 __attribute__((ext_vector_type(4)));
typedef unsigned u32x2 __attribute__((ext_vector_type(2)));
#define LDSP __attribute__((address_space(3)))

#define TSDECL
#define TS(i)
#define TSEND(wsb, kidx)

#define ATROT 0
#define GPRIO 0
#define K3ROT ((2 * nq + (idx >> 2)) & 7)
#define K3CFG 8, 4, 8
#define K2CFG 16, 3, 4
#define ABURST(mt) ((mt) == 2)
#define WT1 true
#define WT2 true
#define WT3 true
#define K2ROT 0
constexpr int SEQ = 2048, DM = 448, NH = 16, DH = 28;

constexpr size_t WS_Q = 0;
constexpr size_t WS_K = 2u << 20;
constexpr size_t WS_V = 4u << 20;
constexpr size_t WS_B = 6u << 20;
constexpr size_t WS_O = 10u << 20;
constexpr size_t WS_P = 12u << 20;

template <int... Is, class F>
__device__ __forceinline__ void static_for_impl(std::integer_sequence<int, Is...>, F&& f) {
  (f(std::integral_constant<int, Is>{}), ...);
}
template <int N, class F>
__device__ __forceinline__ void static_for(F&& f) {
  static_for_impl(std::make_integer_sequence<int, N>{}, static_cast<F&&>(f));
}

__device__ __forceinline__ unsigned pack2(float a, float b) {
  bf16x2 v; v[0] = (__bf16)a; v[1] = (__bf16)b;
  return __builtin_bit_cast(unsigned, v);
}

template <int NKB, int NSA, int NSB, int MT>
struct Sched {
  static constexpr int NS = 2 * NKB;
  static constexpr int VB = 2, VA = 2 * MT;
  static constexpr int PA = NSA >= NKB ? NKB : NSA - 1;
  static constexpr int PB = NSB >= NS ? NS : NSB - 1;
  static constexpr int PM = (PA > (PB + 1) / 2) ? PA : (PB + 1) / 2;
  static constexpr int wait(int s) {
    int pos = 0, endB = 0, endA = 0;
    for (int p = 0; p < PM; ++p) {
      if (p < PA) { pos += VA; if (p == s / 2) endA = pos; }
      if (2 * p < PB) { pos += VB; if (2 * p == s) endB = pos; }
      if (2 * p + 1 < PB) { pos += VB; if (2 * p + 1 == s) endB = pos; }
    }
    for (int it = 0; it < s; ++it) {
      if (it + PB < NS) { pos += VB; if (it + PB == s) endB = pos; }
      if (it / 2 + PA < NKB && !(ABURST(MT) && (it & 1))) { pos += ABURST(MT) ? 2 * MT : MT; if (it / 2 + PA == s / 2) endA = pos; }
    }
    int e = endB > endA ? endB : endA;
    return pos - e;
  }
};

template <bool NT, bool L48>
__device__ __forceinline__ void glds16(unsigned voff, const void* sbase, unsigned lds_dst) {
  unsigned keep;
#define GLDS_(pre, pol, post) \
  asm volatile("s_mov_b32 %0, m0\n\ts_mov_b32 m0, %3\n\t" pre "s_nop 0\n\tglobal_load_lds_dwordx4 %1, %2" pol "\n\t" post "s_mov_b32 m0, %0" \
               : "=&s"(keep) : "v"(voff), "s"(sbase), "s"(lds_dst) : "memory")
  if constexpr (L48) {
    if constexpr (NT) GLDS_("s_mov_b32 exec_hi, 0xffff\n\t", " nt", "s_mov_b32 exec_hi, -1\n\t");
    else GLDS_("s_mov_b32 exec_hi, 0xffff\n\t", "", "s_mov_b32 exec_hi, -1\n\t");
  } else {
    if constexpr (NT) GLDS_("", " nt", "");
    else GLDS_("", "", "");
  }
#undef GLDS_
}
#define NTA(bk) ((bk) == 2)
#define NTB(bk) false

template <bool WT>
__device__ __forceinline__ void st16(void* p, u32x4 v) {
  if constexpr (WT) asm volatile("global_store_dwordx4 %0, %1, off sc1\n\ts_nop 1" ::"v"(p), "v"(v) : "memory");
  else *(u32x4*)p = v;
}

template <int OFF>
__device__ __forceinline__ void gload16(bf16x8& dst, unsigned voff, const void* sbase) {
  asm volatile("global_load_dwordx4 %0, %1, %2 offset:%3" : "=v"(dst) : "v"(voff), "s"(sbase), "i"(OFF) : "memory");
}
__device__ __forceinline__ void tie2(bf16x8& x, bf16x8& y) { asm volatile("" : "+v"(x), "+v"(y)); }

template <int NKB, int NSA, int NSB, int MT, int BKIND, bool SWAP>
__device__ __forceinline__ void gemm_core(const char* Abase, const unsigned (&avoff)[2 * MT], const char* Bbase, int ldb2,
                                          char* smem, int wave, int lane, int rot, f32x4 (&acc)[MT][7]) {
  using SC = Sched<NKB, NSA, NSB, MT>;
  constexpr int NS = SC::NS, PA = SC::PA, PB = SC::PB;
  constexpr int ASLOT = MT * 8192;
  constexpr bool BTR = BKIND == 0 || BKIND == 3, ADIR = BKIND == 3;
  constexpr int BOFF = ADIR ? 0 : NSA * ASLOT;
  const unsigned lds0 = (unsigned)(uintptr_t)(LDSP char*)smem;
  unsigned bvoff[2];
  static_for<2>([&](auto P) {
    constexpr int p = P;
    int L = wave * 1792 + p * 1024 + lane * 16;
    if (p == 1 && lane >= 48) L = wave * 1792;
    if constexpr (BTR) {
      int pos = L / 224, ch = (L - pos * 224) >> 4;
      int k = (pos & 3) + 8 * ((pos >> 2) & 1) + 4 * ((pos >> 3) & 1) + (pos & 16);
      if constexpr (BKIND == 3) k = k < 28 ? k : 27;
      bvoff[p] = k * ldb2 + ch * 16;
    } else if constexpr (BKIND == 1) {
      int n = L >> 6, chp = (L >> 4) & 3;
      bvoff[p] = n * ldb2 + 16 * (chp ^ ((-(n >> 2)) & 3));
    } else {
      bvoff[p] = L;
    }
  });
  const int bstep = (BKIND == 0) ? 32 * ldb2 : (BKIND == 1 ? 64 : (BKIND == 2 ? ldb2 : 28 * ldb2));
  bf16x8 fd[ADIR ? NS : 1][MT];
  auto issueB1 = [&](auto S, auto P) {
    constexpr int s = S;
    int se = s + 2 * rot;
    se = se >= NS ? se - NS : se;
    const char* src = Bbase + (ptrdiff_t)se * bstep;
    const unsigned d = lds0 + BOFF + (s % NSB) * 7168 + wave * 1792;
    if constexpr (P == 0) glds16<NTB(BKIND), false>(bvoff[0], src, d);
    else glds16<NTB(BKIND), true>(bvoff[1], src, d + 1024);
  };
  auto issueA1 = [&](auto KB, auto P) {
    constexpr int kb = KB;
    int ke = kb + rot;
    ke = ke >= NKB ? ke - NKB : ke;
    if constexpr (ADIR) {
      gload16<(P & 1) * 1024>(fd[2 * kb + (P & 1)][P >> 1], avoff[P & ~1], Abase + ke * 2048);
    } else {
      const char* src = Abase + ke * 128;
      const unsigned d = lds0 + (kb % NSA) * ASLOT + wave * (MT * 2048);
      glds16<NTA(BKIND), false>(avoff[P], src, d + P * 1024);
    }
  };
  auto issueB = [&](auto S) { static_for<2>([&](auto P) { issueB1(S, P); }); };
  auto issueA = [&](auto KB) { static_for<2 * MT>([&](auto P) { issueA1(KB, P); }); };
  auto stepload = [&](auto S, auto J) {
    constexpr int s = S, j = J;
    if constexpr (j < 2) {
      if constexpr (s + PB < NS) issueB1(std::integral_constant<int, s + PB>{}, J);
    } else {
      if constexpr (s / 2 + PA < NKB) {
        if constexpr (!ABURST(MT)) {
          issueA1(std::integral_constant<int, s / 2 + PA>{}, std::integral_constant<int, (s & 1) * MT + j - 2>{});
        } else if constexpr ((s & 1) == 0) {
          issueA1(std::integral_constant<int, s / 2 + PA>{}, std::integral_constant<int, 2 * (j - 2)>{});
          issueA1(std::integral_constant<int, s / 2 + PA>{}, std::integral_constant<int, 2 * (j - 2) + 1>{});
        }
      }
    }
  };
  static_for<SC::PM>([&](auto P) {
    constexpr int p = P;
    if constexpr (p < PA) issueA(P);
    if constexpr (2 * p < PB) issueB(std::integral_constant<int, 2 * p>{});
    if constexpr (2 * p + 1 < PB) issueB(std::integral_constant<int, 2 * p + 1>{});
  });
  const int c = lane & 15, g = lane >> 4;
  int rb;
  if constexpr (BTR) rb = ((c >> 2) + 4 * (g & 1) + 16 * (g >> 1)) * 224 + 8 * (c & 3);
  else if constexpr (BKIND == 1) rb = c * 64 + 16 * (g ^ ((-(c >> 2)) & 3));
  else rb = lane * 16;
  const int ra0 = wave * (MT * 2048) + c * 128 + 16 * (g ^ (c >> 1));
  const int ra1 = ra0 ^ 64;

  bf16x8 fa[2][MT], fb[2][7];
  auto sync = [&](auto S) {
    constexpr int s = S;
    asm volatile("s_waitcnt vmcnt(%0) lgkmcnt(0)" ::"i"(SC::wait(s)) : "memory");
    __builtin_amdgcn_s_barrier();
    if constexpr (BKIND == 2 && s == 0 && GPRIO > 0) __builtin_amdgcn_s_setprio(1);
    if constexpr (BKIND == 2 && s == GPRIO && GPRIO > 0) __builtin_amdgcn_s_setprio(0);
    if constexpr (ADIR && (s & 1) == 0) static_for<MT>([&](auto M) { tie2(fd[s][M], fd[s + 1][M]); });
    __builtin_amdgcn_sched_barrier(0);
  };
  constexpr int NRA = ADIR ? 0 : MT;
  constexpr int NSIDE = NRA + 7 + 2 + MT;
  auto side = [&](auto S, auto K) {
    constexpr int s = S, k = K;
    if constexpr (k < NRA) {
      const char* sa = smem + ((s / 2) % NSA) * ASLOT + ((s & 1) ? ra1 : ra0);
      fa[s & 1][k] = *(const bf16x8*)(sa + k * 2048);
    } else if constexpr (k < NRA + 7) {
      constexpr int nt = k - NRA;
      const char* sb = smem + BOFF + (s % NSB) * 7168 + rb;
      if constexpr (BTR) {
        s16x4 lo = __builtin_amdgcn_ds_read_tr16_b64_v4i16((LDSP s16x4*)(sb + 32 * nt));
        s16x4 hi = __builtin_amdgcn_ds_read_tr16_b64_v4i16((LDSP s16x4*)(sb + 8 * 224 + 32 * nt));
        typedef short s16x8 __attribute__((ext_vector_type(8)));
        s16x8 t = __builtin_shufflevector(lo, hi, 0, 1, 2, 3, 4, 5, 6, 7);
        fb[s & 1][nt] = __builtin_bit_cast(bf16x8, t);
      } else {
        fb[s & 1][nt] = *(const bf16x8*)(sb + nt * 1024);
      }
    } else {
      stepload(S, std::integral_constant<int, k - NRA - 7>{});
    }
    __builtin_amdgcn_sched_barrier(0);
  };
  sync(std::integral_constant<int, 0>{});
  static_for<NSIDE>([&](auto K) { side(std::integral_constant<int, 0>{}, K); });
  static_for<NS>([&](auto S) {
    constexpr int s = S;
    if constexpr (s + 1 < NS) sync(std::integral_constant<int, s + 1>{});
    static_for<7>([&](auto N) {
      constexpr int nt = N;
      static_for<MT>([&](auto M) {
        constexpr int m = M;
        const bf16x8 av = ADIR ? fd[ADIR ? s : 0][m] : fa[s & 1][m];
        if constexpr (SWAP)
          acc[m][nt] = __builtin_amdgcn_mfma_f32_16x16x32_bf16(fb[s & 1][nt], av, acc[m][nt], 0, 0, 0);
        else
          acc[m][nt] = __builtin_amdgcn_mfma_f32_16x16x32_bf16(av, fb[s & 1][nt], acc[m][nt], 0, 0, 0);
        __builtin_amdgcn_sched_barrier(0);
        if constexpr (s + 1 < NS) {
          static_for<NSIDE>([&](auto K) {
            if constexpr ((K * 7 * MT) / NSIDE == nt * MT + m) side(std::integral_constant<int, s + 1>{}, K);
          });
        }
      });
    });
  });
}

template <int MT>
__device__ __forceinline__ void a_offsets(unsigned (&avoff)[2 * MT], int m0, int lda2, int lane, int maxrow) {
#pragma unroll
  for (int p = 0; p < 2 * MT; ++p) {
    const int r = 8 * p + (lane >> 3);
    int row = m0 + r;
    row = row < maxrow ? row : maxrow;
    avoff[p] = row * lda2 + 16 * ((lane & 7) ^ ((r >> 1) & 7));
  }
}

template <int MT>
__device__ __forceinline__ void store_rows(const f32x4 (&acc)[MT][7], char* tb, int tstride, char* dst, int ld2, int lane) {
  const int c = lane & 15, g = lane >> 4;
#pragma unroll
  for (int m = 0; m < MT; ++m)
#pragma unroll
    for (int n = 0; n < 7; ++n)
      *(u32x2*)(tb + m * tstride + c * 240 + n * 32 + g * 8) = u32x2{pack2(acc[m][n][0], acc[m][n][1]), pack2(acc[m][n][2], acc[m][n][3])};
#pragma unroll
  for (int m = 0; m < MT; ++m)
#pragma unroll
    for (int i = 0; i < 4; ++i) {
      const int u = i * 64 + lane;
      const int row = (u * 4682) >> 16, ch = u - row * 14;
      if (i < 3 || lane < 32) {
        const u32x4 v = *(const u32x4*)(tb + m * tstride + row * 240 + ch * 16);
        st16<MT == 2 ? WT2 : WT3>(dst + (size_t)(m * 16 + row) * ld2 + ch * 16, v);
      }
    }
}

constexpr int K1_MT = 3;
#define K1CFG 7, 3, 4
__global__ void __launch_bounds__(256, 1)
k1_proj(const __bf16* __restrict__ xa, const __bf16* __restrict__ wqkv, const __bf16* __restrict__ xb,
        const __bf16* __restrict__ whr, const __bf16* __restrict__ whi, char* __restrict__ ws) {
  extern __shared__ __attribute__((aligned(16))) char smem[];
  const int lane = threadIdx.x & 63;
  const int wave = __builtin_amdgcn_readfirstlane(threadIdx.x >> 6);
  const int bid = blockIdx.x;
  TSDECL TS(0); TS(1);
  const int xcd = bid & 7, idx = bid >> 3;
  const int rt = idx / 10, ci = idx - 10 * rt;
  const int ct = (xcd & 1) * 10 + ci;
  const int lim = (xcd >> 1) * 512 + 512;
  const int m0 = (xcd >> 1) * 512 + rt * 192 + wave * 48;
  const int rot = (ci + 3 * rt) % 7;
  const int c = lane & 15, g = lane >> 4;
  unsigned avoff[2 * K1_MT];
  a_offsets<K1_MT>(avoff, m0, DM * 2, lane, lim - 1);
  f32x4 acc[K1_MT][7];
#pragma unroll
  for (int m = 0; m < K1_MT; ++m)
#pragma unroll
    for (int n = 0; n < 7; ++n) acc[m][n] = f32x4{0.f, 0.f, 0.f, 0.f};

  char* tb = smem + wave * 6144;
  if (ct < 8) {
    gemm_core<K1CFG, K1_MT, 0, true>((const char*)xa, avoff, (const char*)wqkv + ct * 224, 3 * DM * 2, smem, wave, lane, rot, acc);
    TS(2);
    const bool isq = ct < 4;
    const float sc = isq ? 0.18898223650461362f * 1.4426950408889634f : 1.0f;
    char* dst = ws + (isq ? WS_Q : WS_K);
    const unsigned padlo = isq ? 0u : 0x3f803f80u, padhi = isq ? 0u : 0x00003f80u;
#pragma unroll
    for (int m = 0; m < K1_MT; ++m) {
      char* st = tb + m * 24576;
#pragma unroll
      for (int nt = 0; nt < 7; ++nt) {
        const int n4 = 4 * nt + g;
        const int hh = (n4 * 37) >> 8;
        const int q4 = n4 - 7 * hh;
        char* p = st + (hh * 4 + (q4 >> 1)) * 256 + c * 16 + (q4 & 1) * 8;
        *(u32x2*)p = u32x2{pack2(acc[m][nt][0] * sc, acc[m][nt][1] * sc), pack2(acc[m][nt][2] * sc, acc[m][nt][3] * sc)};
        if (q4 == 6) *(u32x2*)(p + 8) = u32x2{padlo, padhi};
      }
    }
#pragma unroll
    for (int m = 0; m < K1_MT; ++m) {
      const int t0 = m0 + 16 * m;
      if (t0 < lim) {
#pragma unroll
        for (int hh = 0; hh < 4; ++hh) {
          const u32x4 v = *(const u32x4*)(tb + m * 24576 + hh * 1024 + lane * 16);
          const int lo = isq ? (t0 & 16) * 64 + c * 16 : ((c >> 2) & 1) * 1024 + ((t0 & 16) / 2 + (c >> 3) * 4 + (c & 3)) * 16;
          st16<WT1>(dst + ((4 * (ct & 3) + hh) * 64 + (t0 >> 5)) * 2048 + g * 256 + lo, v);
        }
      }
    }
  } else if (ct < 12) {
    gemm_core<K1CFG, K1_MT, 0, false>((const char*)xa, avoff, (const char*)wqkv + ct * 224, 3 * DM * 2, smem, wave, lane, rot, acc);
    TS(2);
    char* dst = ws + WS_V;
#pragma unroll
    for (int m = 0; m < K1_MT; ++m) {
      char* st = tb + m * 24576;
#pragma unroll
      for (int nt = 0; nt < 7; ++nt) {
        const int nl = 16 * nt + c;
        const int hh = (nl * 147) >> 12;
        const int d = nl - 28 * hh;
        char* p = st + hh * 1024 + (d >> 4) * 512 + (g >> 1) * 256 + (d & 15) * 16 + (g & 1) * 8;
        *(u32x2*)p = u32x2{pack2(acc[m][nt][0], acc[m][nt][1]), pack2(acc[m][nt][2], acc[m][nt][3])};
      }
      if (lane < 32) {
        const unsigned o1 = (lane & 3) == 0 ? 0x3f803f80u : 0u;
        *(u32x4*)(st + (lane >> 3) * 1024 + 512 + ((lane >> 2) & 1) * 256 + (12 + (lane & 3)) * 16) = u32x4{o1, o1, o1, o1};
      }
    }
#pragma unroll
    for (int m = 0; m < K1_MT; ++m) {
      const int t0 = m0 + 16 * m;
      if (t0 < lim) {
#pragma unroll
        for (int hh = 0; hh < 4; ++hh) {
          const u32x4 v = *(const u32x4*)(tb + m * 24576 + hh * 1024 + lane * 16);
          st16<WT1>(dst + ((4 * (ct & 3) + hh) * 64 + (t0 >> 5)) * 2048 + (lane >> 5) * 1024 + (t0 & 16) * 32 + (lane & 31) * 16, v);
        }
      }
    }
  } else {
    const bool isb = ct >= 16;
    const int nq = ct & 3;
    const char* W = (const char*)(isb ? whi : whr) + (size_t)nq * 112 * (DM * 2);
    gemm_core<K1CFG, K1_MT, 1, false>((const char*)xb, avoff, W, DM * 2, smem, wave, lane, rot, acc);
    TS(2);
    const float sg = isb ? -1.f : 1.f;
    char* dst = ws + WS_B;
#pragma unroll
    for (int m = 0; m < K1_MT; ++m) {
      char* st = tb + m * 24576;
#pragma unroll
      for (int nt = 0; nt < 7; ++nt)
        *(u32x2*)(st + nt * 512 + (g >> 1) * 256 + c * 16 + (g & 1) * 8) =
            u32x2{pack2(acc[m][nt][0] * sg, acc[m][nt][1] * sg), pack2(acc[m][nt][2] * sg, acc[m][nt][3] * sg)};
    }
#pragma unroll
    for (int m = 0; m < K1_MT; ++m) {
      const int t0 = m0 + 16 * m;
      if (t0 < lim) {
        const int kk = t0 + (isb ? SEQ : 0);
        char* gp = dst + ((kk >> 5) * 4 + nq) * 7168 + (kk & 16) * 32 + (lane >> 5) * 1024 + (lane & 31) * 16;
#pragma unroll
        for (int i = 0; i < 4; ++i) {
          if (i < 3 || lane < 32) {
            const u32x4 v = *(const u32x4*)(tb + m * 24576 + i * 1024 + lane * 16);
            st16<WT1>(gp + i * 2048, v);
          }
        }
      }
    }
  }
  TSEND(ws, 0);
}

__device__ __forceinline__ float col_max(const f32x4& a, const f32x4& b) {
  float m = fmaxf(fmaxf(fmaxf(a[0], a[1]), fmaxf(a[2], a[3])), fmaxf(fmaxf(b[0], b[1]), fmaxf(b[2], b[3])));
  auto r = __builtin_amdgcn_permlane32_swap(__builtin_bit_cast(unsigned, m), __builtin_bit_cast(unsigned, m), false, false);
  m = fmaxf(__builtin_bit_cast(float, (unsigned)r[0]), __builtin_bit_cast(float, (unsigned)r[1]));
  auto t = __builtin_amdgcn_permlane16_swap(__builtin_bit_cast(unsigned, m), __builtin_bit_cast(unsigned, m), false, false);
  return fmaxf(__builtin_bit_cast(float, (unsigned)t[0]), __builtin_bit_cast(float, (unsigned)t[1]));
}
__device__ __forceinline__ unsigned pack2p(float a, float b) {
  unsigned r = pack2(a, b);
  asm volatile("" : "+v"(r));
  return r;
}
#define MFMA16(a, b, c) __builtin_amdgcn_mfma_f32_16x16x32_bf16(a, b, c, 0, 0, 0)

__device__ __forceinline__ void attn_role(const char* __restrict__ ws, char* __restrict__ wso, char* smem, int wg, int wave, int lane) {
  TSDECL TS(0);
  const int xcd = wg & 7, idx = wg >> 3;
  const int head = xcd * 2 + (idx >> 4), qb = idx & 15;
  const bf16x8* Kf = (const bf16x8*)(ws + WS_K) + (size_t)(head * 64 + wave * 16) * 128 + lane;
  const bf16x8* Vf = (const bf16x8*)(ws + WS_V) + (size_t)(head * 64 + wave * 16) * 128 + lane;
  const f32x4 zero4 = {0.f, 0.f, 0.f, 0.f};

  const char* Kb = (const char*)(ws + WS_K) + (size_t)(head * 64 + wave * 16) * 2048;
  const unsigned l16 = lane * 16;
  bf16x8 kf[3][2], vf[3][2];
  bf16x8 qf[8];
  auto loadkv = [&](auto T) {
    constexpr int t = T;
    const char* src = Kb + t * 2048;
    gload16<0>(kf[t % 3][0], l16, src);
    gload16<1024>(kf[t % 3][1], l16, src);
    gload16<0>(vf[t % 3][0], l16, src + (WS_V - WS_K));
    gload16<1024>(vf[t % 3][1], l16, src + (WS_V - WS_K));
  };
  loadkv(std::integral_constant<int, 0>{});
  {
    const char* Qb = (const char*)(ws + WS_Q) + (size_t)(head * 64 + qb * 4) * 2048;
    static_for<4>([&](auto QT) {
      gload16<0>(qf[2 * QT], l16, Qb + QT * 2048);
      gload16<1024>(qf[2 * QT + 1], l16, Qb + QT * 2048);
    });
  }
  loadkv(std::integral_constant<int, 1>{});
  asm volatile("s_waitcnt vmcnt(4)" ::: "memory");
  tie2(kf[0][0], kf[0][1]); tie2(vf[0][0], vf[0][1]);
#pragma unroll
  for (int j = 0; j < 4; ++j) tie2(qf[2 * j], qf[2 * j + 1]);
  const bool top = lane >= 48;
  float mref[8];
#pragma unroll
  for (int j = 0; j < 8; ++j) {
    const f32x4 sa = MFMA16(kf[0][0], qf[j], zero4), sb = MFMA16(kf[0][1], qf[j], zero4);
    const float nm = -(col_max(sa, sb) + 64.f);
    const __bf16 b0 = (__bf16)nm;
    const float r1 = nm - (float)b0;
    const __bf16 b1 = (__bf16)r1;
    const float r2 = r1 - (float)b1;
    const __bf16 b2 = (__bf16)r2;
    mref[j] = -(((float)b0 + (float)b1) + (float)b2);
    if (top) { qf[j][4] = b0; qf[j][5] = b1; qf[j][6] = b2; }
  }
  TS(1);
  f32x4 o[8][2];
#pragma unroll
  for (int j = 0; j < 8; ++j) { o[j][0] = zero4; o[j][1] = zero4; }

  {
#define SB() __builtin_amdgcn_sched_barrier(0)
    f32x4 s0[2], s1[2];
    u32x4 p[2];
    float ea[2], eb[2];
    s0[0] = MFMA16(kf[0][0], qf[0], zero4);
    s1[0] = MFMA16(kf[0][1], qf[0], zero4);
    SB();
    static_for<128>([&](auto I) {
      constexpr int i = I, kt = i >> 3, j = i & 7;
      constexpr int ni = i + 1, nkt = ni >> 3, nj = ni & 7;
      constexpr int pi = i - 1, pkt = pi >> 3, pj = pi & 7;
      if constexpr (j == 7 && ni < 128) {
        asm volatile("s_waitcnt vmcnt(%0)" ::"i"(kt + 2 < 16 ? 4 : 0) : "memory");
        tie2(kf[nkt % 3][0], kf[nkt % 3][1]); tie2(vf[nkt % 3][0], vf[nkt % 3][1]);
      }
      if constexpr (ni < 128) s0[ni & 1] = MFMA16(kf[nkt % 3][0], qf[nj], zero4);
      SB();
      if constexpr (i > 0) p[pi & 1][3] = pack2p(eb[0], eb[1]);
      SB();
      ea[0] = __builtin_amdgcn_exp2f(s0[i & 1][0]); ea[1] = __builtin_amdgcn_exp2f(s0[i & 1][1]);
      SB();
      if constexpr (i > 0) o[pj][0] = MFMA16(vf[pkt % 3][0], __builtin_bit_cast(bf16x8, p[pi & 1]), o[pj][0]);
      SB();
      p[i & 1][0] = pack2p(ea[0], ea[1]);
      SB();
      eb[0] = __builtin_amdgcn_exp2f(s0[i & 1][2]); eb[1] = __builtin_amdgcn_exp2f(s0[i & 1][3]);
      if constexpr (j == 1 && kt + 2 < 16) loadkv(std::integral_constant<int, kt + 2>{});
      SB();
      if constexpr (ni < 128) s1[ni & 1] = MFMA16(kf[nkt % 3][1], qf[nj], zero4);
      SB();
      p[i & 1][1] = pack2p(eb[0], eb[1]);
      SB();
      ea[0] = __builtin_amdgcn_exp2f(s1[i & 1][0]); ea[1] = __builtin_amdgcn_exp2f(s1[i & 1][1]);
      SB();
      if constexpr (i > 0) o[pj][1] = MFMA16(vf[pkt % 3][1], __builtin_bit_cast(bf16x8, p[pi & 1]), o[pj][1]);
      SB();
      p[i & 1][2] = pack2p(ea[0], ea[1]);
      SB();
      eb[0] = __builtin_amdgcn_exp2f(s1[i & 1][2]); eb[1] = __builtin_amdgcn_exp2f(s1[i & 1][3]);
      SB();
    });
    p[1][3] = pack2(eb[0], eb[1]);
    o[7][0] = MFMA16(vf[15 % 3][0], __builtin_bit_cast(bf16x8, p[1]), o[7][0]);
    o[7][1] = MFMA16(vf[15 % 3][1], __builtin_bit_cast(bf16x8, p[1]), o[7][1]);
  }

  TS(2);
  bool bad = false;
  {
    float am = 0.f;
#pragma unroll
    for (int j = 0; j < 8; ++j) {
      const float l = o[j][1][0];
      bad |= top && !(l >= 0x1p-80f && l <= 0x1p64f);
#pragma unroll
      for (int e = 0; e < 4; ++e) am += fabsf(o[j][0][e]) + fabsf(o[j][1][e]);
    }
    bad |= !(am <= 0x1p120f);
  }
  if (__any(bad)) {
#pragma unroll
    for (int j = 0; j < 8; ++j) {
      if (top) { qf[j][4] = (__bf16)0.f; qf[j][5] = (__bf16)0.f; qf[j][6] = (__bf16)0.f; }
      o[j][0] = zero4; o[j][1] = zero4;
      mref[j] = -INFINITY;
    }
#pragma unroll 1
    for (int kt = 0; kt < 16; ++kt) {
      const bf16x8 k0 = Kf[kt * 128], k1 = Kf[kt * 128 + 64], v0 = Vf[kt * 128], v1 = Vf[kt * 128 + 64];
#pragma unroll
      for (int j = 0; j < 8; ++j) {
        const f32x4 sa = MFMA16(k0, qf[j], zero4), sb = MFMA16(k1, qf[j], zero4);
        const float mn = fmaxf(mref[j], col_max(sa, sb));
        const float al = __builtin_amdgcn_exp2f(mref[j] - mn);
        mref[j] = mn;
        bf16x8 pp;
#pragma unroll
        for (int e = 0; e < 4; ++e) {
          pp[e] = (__bf16)__builtin_amdgcn_exp2f(sa[e] - mn);
          pp[4 + e] = (__bf16)__builtin_amdgcn_exp2f(sb[e] - mn);
          o[j][0][e] *= al; o[j][1][e] *= al;
        }
        o[j][0] = MFMA16(v0, pp, o[j][0]);
        o[j][1] = MFMA16(v1, pp, o[j][1]);
      }
    }
  }

  f32x4* part = (f32x4*)smem;
  float* mbuf = (float*)(smem + 65536);
#pragma unroll
  for (int j = 0; j < 8; ++j) {
    mbuf[(j * 4 + wave) * 64 + lane] = top ? mref[j] + __builtin_amdgcn_logf(o[j][1][0]) : mref[j];
    if ((j >> 1) != wave) {
      part[((j * 4 + wave) * 2 + 0) * 64 + lane] = o[j][0];
      part[((j * 4 + wave) * 2 + 1) * 64 + lane] = o[j][1];
    }
  }
  f32x4 mine[2][2];
#pragma unroll
  for (int jj = 0; jj < 2; ++jj)
#pragma unroll
    for (int dt = 0; dt < 2; ++dt)
      mine[jj][dt] = wave == 0 ? o[jj][dt] : (wave == 1 ? o[2 + jj][dt] : (wave == 2 ? o[4 + jj][dt] : o[6 + jj][dt]));
  __syncthreads();
  const int c = lane & 15, g = lane >> 4;
#pragma unroll
  for (int jj = 0; jj < 2; ++jj) {
    const int j = 2 * wave + jj;
    float mw[4];
#pragma unroll
    for (int w = 0; w < 4; ++w) mw[w] = mbuf[(j * 4 + w) * 64 + 48 + c];
    const float mm = fmaxf(fmaxf(mw[0], mw[1]), fmaxf(mw[2], mw[3]));
#pragma unroll
    for (int w = 0; w < 4; ++w) mw[w] = mbuf[(j * 4 + w) * 64 + c];
    f32x4 r[2] = {zero4, zero4};
#pragma unroll
    for (int w = 0; w < 4; ++w) {
      const float f = __builtin_amdgcn_exp2f(mw[w] - mm);
#pragma unroll
      for (int dt = 0; dt < 2; ++dt) {
        f32x4 t;
        if (w == wave) t = mine[jj][dt];
        else t = part[((j * 4 + w) * 2 + dt) * 64 + lane];
#pragma unroll
        for (int e = 0; e < 4; ++e) r[dt][e] += f * t[e];
      }
    }
    const float inv = 1.0f / __shfl(r[1][0], 48 + c);
    const unsigned x0 = pack2(r[0][0] * inv, r[0][1] * inv), x1 = pack2(r[0][2] * inv, r[0][3] * inv);
    unsigned y0 = pack2(r[1][0] * inv, r[1][1] * inv), y1 = pack2(r[1][2] * inv, r[1][3] * inv);
    if (top) { y0 = 0u; y1 = 0u; }
    auto sa = __builtin_amdgcn_permlane16_swap(x0, y0, false, false);
    auto sb = __builtin_amdgcn_permlane16_swap(x1, y1, false, false);
    char* op = wso + WS_O + ((size_t)(qb * 8 + j) * 16 + head) * 1024 + ((g & 1) * 2 + (g >> 1)) * 256 + c * 16;
    st16<WT2>(op, u32x4{(unsigned)sa[0], (unsigned)sb[0], (unsigned)sa[1], (unsigned)sb[1]});
  }
  TSEND(wso, 1);
}

__global__ void __launch_bounds__(256, 2)
k2_main(const __bf16* __restrict__ csr, const __bf16* __restrict__ csi, char* __restrict__ ws) {
  extern __shared__ __attribute__((aligned(16))) char smem[];
  const int lane = threadIdx.x & 63;
  const int wave = __builtin_amdgcn_readfirstlane(threadIdx.x >> 6);
  const int bid = blockIdx.x;
  if (bid < 256) {
    attn_role(ws, ws, smem, bid, wave, lane);
  } else {
    TSDECL TS(0); TS(1);
    const int wg = bid - 256;
    const int xcd = wg & 7, idx = wg >> 3;
    const int ks = xcd >> 1, mt = (xcd & 1) * 8 + (idx >> 2), nq = idx & 3;
    const int m0 = mt * 128 + wave * 32;
    unsigned avoff[4];
    a_offsets<2>(avoff, m0, SEQ * 2, lane, SEQ - 1);
    const char* A = (const char*)(ks < 2 ? csr : csi) + (ks & 1) * 2048;
    const char* B = ws + WS_B + ((size_t)(ks * 32) * 4 + nq) * 7168;
    f32x4 acc[2][7];
#pragma unroll
    for (int m = 0; m < 2; ++m)
#pragma unroll
      for (int n = 0; n < 7; ++n) acc[m][n] = f32x4{0.f, 0.f, 0.f, 0.f};
    gemm_core<K2CFG, 2, 2, true>(A, avoff, B, 4 * 7168, smem, wave, lane, K2ROT, acc);
    TS(2);
    store_rows<2>(acc, smem + wave * 4096, 16384, ws + WS_P + ((size_t)ks * SEQ + m0) * (DM * 2) + nq * 224, DM * 2, lane);
    TSEND(ws, 1);
  }
}

__global__ void __launch_bounds__(256, 1)
k3_final(const __bf16* __restrict__ wo, const char* __restrict__ ws, __bf16* __restrict__ out) {
  extern __shared__ __attribute__((aligned(16))) char smem[];
  const int lane = threadIdx.x & 63;
  const int wave = __builtin_amdgcn_readfirstlane(threadIdx.x >> 6);
  const int bid = blockIdx.x;
  TSDECL TS(0); TS(1); TS(2);
  const int xcd = bid & 7, idx = bid >> 3;
  if (idx < 16) {
    const int rt = xcd * 4 + (idx >> 2), nq = idx & 3;
    const int m0 = rt * 64 + wave * 16;
    const unsigned avoff[2] = {(unsigned)(lane * 16), 0u};
    f32x4 acc[1][7];
#pragma unroll
    for (int n = 0; n < 7; ++n) acc[0][n] = f32x4{0.f, 0.f, 0.f, 0.f};
    gemm_core<K3CFG, 1, 3, true>(ws + WS_O + (size_t)(m0 >> 4) * 16384, avoff, (const char*)wo + nq * 224, DM * 2, smem, wave, lane,
                                    K3ROT, acc);
    store_rows<1>(acc, smem + 16 * 7168 + wave * 4096, 0, (char*)out + (size_t)m0 * (2 * DM * 2) + nq * 224, 2 * DM * 2, lane);
  } else {
    const int u0 = (xcd * 14 + idx - 16) * 1024 + threadIdx.x;
    u32x4 t[4][4];
#pragma unroll
    for (int n = 0; n < 4; ++n)
#pragma unroll
      for (int ks = 0; ks < 4; ++ks) t[n][ks] = *(const u32x4*)(ws + WS_P + (size_t)ks * (SEQ * DM * 2) + (size_t)(u0 + n * 256) * 16);
#pragma unroll
    for (int n = 0; n < 4; ++n) {
      u32x4 r;
#pragma unroll
      for (int e = 0; e < 4; ++e) {
        float lo = 0.f, hi = 0.f;
#pragma unroll
        for (int ks = 0; ks < 4; ++ks) {
          lo += __builtin_bit_cast(float, t[n][ks][e] << 16);
          hi += __builtin_bit_cast(float, t[n][ks][e] & 0xffff0000u);
        }
        r[e] = pack2(lo, hi);
      }
      const int u = u0 + n * 256, row = u / 56, ch = u - row * 56;
      st16<WT3>((char*)out + (size_t)row * (2 * DM * 2) + DM * 2 + ch * 16, r);
    }
  }
  TSEND(ws, 2);
}

extern "C" void kernel_launch(void* const* d_in, const int* in_sizes, int n_in, void* d_out, int out_size,
                              void* d_ws, size_t ws_size, hipStream_t stream) {
  const __bf16* xa = (const __bf16*)d_in[0];
  const __bf16* wqkv = (const __bf16*)d_in[1];
  const __bf16* wo = (const __bf16*)d_in[2];
  const __bf16* xb = (const __bf16*)d_in[3];
  const __bf16* csr = (const __bf16*)d_in[4];
  const __bf16* csi = (const __bf16*)d_in[5];
  const __bf16* whr = (const __bf16*)d_in[6];
  const __bf16* whi = (const __bf16*)d_in[7];
  char* ws = (char*)d_ws;
  constexpr int L1 = 3 * 24576 + 4 * 7168, L2 = 81920, L3 = 16 * 7168 + 4 * 4096;
  (void)hipFuncSetAttribute((const void*)k1_proj, hipFuncAttributeMaxDynamicSharedMemorySize, L1);
  (void)hipFuncSetAttribute((const void*)k2_main, hipFuncAttributeMaxDynamicSharedMemorySize, L2);
  (void)hipFuncSetAttribute((const void*)k3_final, hipFuncAttributeMaxDynamicSharedMemorySize, L3);
  k1_proj<<<240, 256, L1, stream>>>(xa, wqkv, xb, whr, whi, ws);
  k2_main<<<512, 256, L2, stream>>>(csr, csi, ws);
  k3_final<<<240, 256, L3, stream>>>(wo, ws, (__bf16*)d_out);
}
```

```cpp
#include <hip/hip_runtime.h>
#include <hip/hip_bf16.h>
#include <stdint.h>
#include <utility>

typedef __bf16 bf16x8 __attribute__((ext_vector_type(8)));
typedef __bf16 bf16x4 __attribute__((ext_vector_type(4)));
typedef __bf16 bf16x2 __attribute__((ext_vector_type(2)));
typedef short s16x4 __attribute__((ext_vector_type(4)));
typedef float f32x4 __attribute__((ext_vector_type(4)));
typedef float f32x16 __attribute__((ext_vector_type(16)));
typedef unsigned u32x4 __attribute__((ext_vector_type(4)));
typedef unsigned u32x2 __attribute__((ext_vector_type(2)));
#define LDSP __attribute__((address_space(3)))

#define TSDECL
#define TS(i)
#define TSEND(wsb, kidx)

#define ATROT 0
#define GPRIO 0
#define K3ROT ((2 * nq + (idx >> 2)) & 7)
#define K3CFG 8, 4, 8
#define K2CFG 16, 3, 4
#define ABURST(mt) ((mt) == 2)
#define WT1 true
#define WT2 true
#define WT3 true
#define XSLOT 1
#define K2ROT 0
constexpr int SEQ = 2048, DM = 448, NH = 16, DH = 28;

constexpr size_t WS_Q = 0;
constexpr size_t WS_K = 2u << 20;
constexpr size_t WS_V = 4u << 20;
constexpr size_t WS_B = 6u << 20;
constexpr size_t WS_O = 10u << 20;
constexpr size_t WS_P = 12u << 20;

template <int... Is, class F>
__device__ __forceinline__ void static_for_impl(std::integer_sequence<int, Is...>, F&& f) {
  (f(std::integral_constant<int, Is>{}), ...);
}
template <int N, class F>
__device__ __forceinline__ void static_for(F&& f) {
  static_for_impl(std::make_integer_sequence<int, N>{}, static_cast<F&&>(f));
}

__device__ __forceinline__ unsigned pack2(float a, float b) {
  bf16x2 v; v[0] = (__bf16)a; v[1] = (__bf16)b;
  return __builtin_bit_cast(unsigned, v);
}

template <int NKB, int NSA, int NSB, int MT>
struct Sched {
  static constexpr int NS = 2 * NKB;
  static constexpr int VB = 2, VA = 2 * MT;
  static constexpr int PA = NSA >= NKB ? NKB : NSA - 1;
  static constexpr int PB = NSB >= NS ? NS : NSB - 1;
  static constexpr int PM = (PA > (PB + 1) / 2) ? PA : (PB + 1) / 2;
  static constexpr int wait(int s) {
    int pos = 0, endB = 0, endA = 0;
    for (int p = 0; p < PM; ++p) {
      if (p < PA) { pos += VA; if (p == s / 2) endA = pos; }
      if (2 * p < PB) { pos += VB; if (2 * p == s) endB = pos; }
      if (2 * p + 1 < PB) { pos += VB; if (2 * p + 1 == s) endB = pos; }
    }
    for (int it = 0; it < s; ++it) {
      if (it + PB < NS) { pos += VB; if (it + PB == s) endB = pos; }
      if (it / 2 + PA < NKB && !(ABURST(MT) && (it & 1))) { pos += ABURST(MT) ? 2 * MT : MT; if (it / 2 + PA == s / 2) endA = pos; }
    }
    int e = endB > endA ? endB : endA;
    return pos - e;
  }
};

template <bool NT, bool L48>
__device__ __forceinline__ void glds16(unsigned voff, const void* sbase, unsigned lds_dst) {
  unsigned keep;
#define GLDS_(pre, pol, post) \
  asm volatile("s_mov_b32 %0, m0\n\ts_mov_b32 m0, %3\n\t" pre "s_nop 0\n\tglobal_load_lds_dwordx4 %1, %2" pol "\n\t" post "s_mov_b32 m0, %0" \
               : "=&s"(keep) : "v"(voff), "s"(sbase), "s"(lds_dst) : "memory")
  if constexpr (L48) {
    if constexpr (NT) GLDS_("s_mov_b32 exec_hi, 0xffff\n\t", " nt", "s_mov_b32 exec_hi, -1\n\t");
    else GLDS_("s_mov_b32 exec_hi, 0xffff\n\t", "", "s_mov_b32 exec_hi, -1\n\t");
  } else {
    if constexpr (NT) GLDS_("", " nt", "");
    else GLDS_("", "", "");
  }
#undef GLDS_
}
#define NTA(bk) ((bk) == 2)
#define NTB(bk) false

template <bool WT>
__device__ __forceinline__ void st16(void* p, u32x4 v) {
  if constexpr (WT) asm volatile("global_store_dwordx4 %0, %1, off sc1\n\ts_nop 1" ::"v"(p), "v"(v) : "memory");
  else *(u32x4*)p = v;
}

__device__ __forceinline__ void touch(unsigned& sink, const void* p) {
  asm volatile("global_load_dword %0, %1, off" : "=v"(sink) : "v"(p) : "memory");
}
__device__ __forceinline__ void keep(unsigned sink) { asm volatile("" ::"v"(sink)); }

template <int OFF>
__device__ __forceinline__ void gload16(bf16x8& dst, unsigned voff, const void* sbase) {
  asm volatile("global_load_dwordx4 %0, %1, %2 offset:%3" : "=v"(dst) : "v"(voff), "s"(sbase), "i"(OFF) : "memory");
}
__device__ __forceinline__ void tie2(bf16x8& x, bf16x8& y) { asm volatile("" : "+v"(x), "+v"(y)); }

template <int NKB, int NSA, int NSB, int MT, int BKIND, bool SWAP>
__device__ __forceinline__ void gemm_core(const char* Abase, const unsigned (&avoff)[2 * MT], const char* Bbase, int ldb2,
                                          char* smem, int wave, int lane, int rot, f32x4 (&acc)[MT][7]) {
  using SC = Sched<NKB, NSA, NSB, MT>;
  constexpr int NS = SC::NS, PA = SC::PA, PB = SC::PB;
  constexpr int ASLOT = MT * 8192;
  constexpr bool BTR = BKIND == 0 || BKIND == 3, ADIR = BKIND == 3;
  constexpr int BOFF = ADIR ? 0 : NSA * ASLOT;
  constexpr int NSLB = ADIR ? NS : (MT == 3 ? NSB + XSLOT : NSB);
  const unsigned lds0 = (unsigned)(uintptr_t)(LDSP char*)smem;
  unsigned bvoff[2];
  static_for<2>([&](auto P) {
    constexpr int p = P;
    int L = wave * 1792 + p * 1024 + lane * 16;
    if (p == 1 && lane >= 48) L = wave * 1792;
    if constexpr (BTR) {
      int pos = L / 224, ch = (L - pos * 224) >> 4;
      int k = (pos & 3) + 8 * ((pos >> 2) & 1) + 4 * ((pos >> 3) & 1) + (pos & 16);
      if constexpr (BKIND == 3) k = k < 28 ? k : 27;
      bvoff[p] = k * ldb2 + ch * 16;
    } else if constexpr (BKIND == 1) {
      int n = L >> 6, chp = (L >> 4) & 3;
      bvoff[p] = n * ldb2 + 16 * (chp ^ ((-(n >> 2)) & 3));
    } else {
      bvoff[p] = L;
    }
  });
  const int bstep = (BKIND == 0) ? 32 * ldb2 : (BKIND == 1 ? 64 : (BKIND == 2 ? ldb2 : 28 * ldb2));
  bf16x8 fd[ADIR ? NS : 1][MT];
  auto issueB1 = [&](auto S, auto P) {
    constexpr int s = S;
    int se = s + 2 * rot;
    se = se >= NS ? se - NS : se;
    const char* src = Bbase + (ptrdiff_t)se * bstep;
    const unsigned d = lds0 + BOFF + (s % NSLB) * 7168 + wave * 1792;
    if constexpr (P == 0) glds16<NTB(BKIND), false>(bvoff[0], src, d);
    else glds16<NTB(BKIND), true>(bvoff[1], src, d + 1024);
  };
  auto issueA1 = [&](auto KB, auto P) {
    constexpr int kb = KB;
    int ke = kb + rot;
    ke = ke >= NKB ? ke - NKB : ke;
    if constexpr (ADIR) {
      gload16<(P & 1) * 1024>(fd[2 * kb + (P & 1)][P >> 1], avoff[P & ~1], Abase + ke * 2048);
    } else {
      const char* src = Abase + ke * 128;
      const unsigned d = lds0 + (kb % NSA) * ASLOT + wave * (MT * 2048);
      glds16<NTA(BKIND), false>(avoff[P], src, d + P * 1024);
    }
  };
  auto issueB = [&](auto S) { static_for<2>([&](auto P) { issueB1(S, P); }); };
  auto issueA = [&](auto KB) { static_for<2 * MT>([&](auto P) { issueA1(KB, P); }); };
  auto stepload = [&](auto S, auto J) {
    constexpr int s = S, j = J;
    if constexpr (j < 2) {
      if constexpr (s + PB < NS) issueB1(std::integral_constant<int, s + PB>{}, J);
    } else {
      if constexpr (s / 2 + PA < NKB) {
        if constexpr (!ABURST(MT)) {
          issueA1(std::integral_constant<int, s / 2 + PA>{}, std::integral_constant<int, (s & 1) * MT + j - 2>{});
        } else if constexpr ((s & 1) == 0) {
          issueA1(std::integral_constant<int, s / 2 + PA>{}, std::integral_constant<int, 2 * (j - 2)>{});
          issueA1(std::integral_constant<int, s / 2 + PA>{}, std::integral_constant<int, 2 * (j - 2) + 1>{});
        }
      }
    }
  };
  static_for<SC::PM>([&](auto P) {
    constexpr int p = P;
    if constexpr (p < PA) issueA(P);
    if constexpr (2 * p < PB) issueB(std::integral_constant<int, 2 * p>{});
    if constexpr (2 * p + 1 < PB) issueB(std::integral_constant<int, 2 * p + 1>{});
  });
  const int c = lane & 15, g = lane >> 4;
  int rb;
  if constexpr (BTR) rb = ((c >> 2) + 4 * (g & 1) + 16 * (g >> 1)) * 224 + 8 * (c & 3);
  else if constexpr (BKIND == 1) rb = c * 64 + 16 * (g ^ ((-(c >> 2)) & 3));
  else rb = lane * 16;
  const int ra0 = wave * (MT * 2048) + c * 128 + 16 * (g ^ (c >> 1));
  const int ra1 = ra0 ^ 64;

  bf16x8 fa[2][MT], fb[2][7];
  auto sync = [&](auto S) {
    constexpr int s = S;
    if constexpr (NSLB > NSB) asm volatile("s_waitcnt vmcnt(%0)" ::"i"(SC::wait(s)) : "memory");
    else asm volatile("s_waitcnt vmcnt(%0) lgkmcnt(0)" ::"i"(SC::wait(s)) : "memory");
    __builtin_amdgcn_s_barrier();
    if constexpr (BKIND == 2 && s == 0 && GPRIO > 0) __builtin_amdgcn_s_setprio(1);
    if constexpr (BKIND == 2 && s == GPRIO && GPRIO > 0) __builtin_amdgcn_s_setprio(0);
    if constexpr (ADIR && (s & 1) == 0) static_for<MT>([&](auto M) { tie2(fd[s][M], fd[s + 1][M]); });
    __builtin_amdgcn_sched_barrier(0);
  };
  constexpr int NRA = ADIR ? 0 : MT;
  constexpr int NSIDE = NRA + 7 + 2 + MT;
  auto side = [&](auto S, auto K) {
    constexpr int s = S, k = K;
    if constexpr (k < NRA) {
      const char* sa = smem + ((s / 2) % NSA) * ASLOT + ((s & 1) ? ra1 : ra0);
      fa[s & 1][k] = *(const bf16x8*)(sa + k * 2048);
    } else if constexpr (k < NRA + 7) {
      constexpr int nt = k - NRA;
      const char* sb = smem + BOFF + (s % NSLB) * 7168 + rb;
      if constexpr (BTR) {
        s16x4 lo = __builtin_amdgcn_ds_read_tr16_b64_v4i16((LDSP s16x4*)(sb + 32 * nt));
        s16x4 hi = __builtin_amdgcn_ds_read_tr16_b64_v4i16((LDSP s16x4*)(sb + 8 * 224 + 32 * nt));
        typedef short s16x8 __attribute__((ext_vector_type(8)));
        s16x8 t = __builtin_shufflevector(lo, hi, 0, 1, 2, 3, 4, 5, 6, 7);
        fb[s & 1][nt] = __builtin_bit_cast(bf16x8, t);
      } else {
        fb[s & 1][nt] = *(const bf16x8*)(sb + nt * 1024);
      }
    } else {
      stepload(S, std::integral_constant<int, k - NRA - 7>{});
    }
    __builtin_amdgcn_sched_barrier(0);
  };
  sync(std::integral_constant<int, 0>{});
  static_for<NSIDE>([&](auto K) { side(std::integral_constant<int, 0>{}, K); });
  static_for<NS>([&](auto S) {
    constexpr int s = S;
    if constexpr (s + 1 < NS) sync(std::integral_constant<int, s + 1>{});
    static_for<7>([&](auto N) {
      constexpr int nt = N;
      static_for<MT>([&](auto M) {
        constexpr int m = M;
        const bf16x8 av = ADIR ? fd[ADIR ? s : 0][m] : fa[s & 1][m];
        if constexpr (SWAP)
          acc[m][nt] = __builtin_amdgcn_mfma_f32_16x16x32_bf16(fb[s & 1][nt], av, acc[m][nt], 0, 0, 0);
        else
          acc[m][nt] = __builtin_amdgcn_mfma_f32_16x16x32_bf16(av, fb[s & 1][nt], acc[m][nt], 0, 0, 0);
        __builtin_amdgcn_sched_barrier(0);
        if constexpr (s + 1 < NS) {
          static_for<NSIDE>([&](auto K) {
            if constexpr ((K * 7 * MT) / NSIDE == nt * MT + m) side(std::integral_constant<int, s + 1>{}, K);
          });
        }
      });
    });
  });
}

template <int MT>
__device__ __forceinline__ void a_offsets(unsigned (&avoff)[2 * MT], int m0, int lda2, int lane, int maxrow) {
#pragma unroll
  for (int p = 0; p < 2 * MT; ++p) {
    const int r = 8 * p + (lane >> 3);
    int row = m0 + r;
    row = row < maxrow ? row : maxrow;
    avoff[p] = row * lda2 + 16 * ((lane & 7) ^ ((r >> 1) & 7));
  }
}

template <int MT>
__device__ __forceinline__ void store_rows(const f32x4 (&acc)[MT][7], char* tb, int tstride, char* dst, int ld2, int lane) {
  const int c = lane & 15, g = lane >> 4;
#pragma unroll
  for (int m = 0; m < MT; ++m)
#pragma unroll
    for (int n = 0; n < 7; ++n)
      *(u32x2*)(tb + m * tstride + c * 240 + n * 32 + g * 8) = u32x2{pack2(acc[m][n][0], acc[m][n][1]), pack2(acc[m][n][2], acc[m][n][3])};
#pragma unroll
  for (int m = 0; m < MT; ++m)
#pragma unroll
    for (int i = 0; i < 4; ++i) {
      const int u = i * 64 + lane;
      const int row = (u * 4682) >> 16, ch = u - row * 14;
      if (i < 3 || lane < 32) {
        const u32x4 v = *(const u32x4*)(tb + m * tstride + row * 240 + ch * 16);
        st16<MT == 2 ? WT2 : WT3>(dst + (size_t)(m * 16 + row) * ld2 + ch * 16, v);
      }
    }
}

constexpr int K1_MT = 3;
#define K1CFG 7, 3, 4
__global__ void __launch_bounds__(256, 1)
k1_proj(const __bf16* __restrict__ xa, const __bf16* __restrict__ wqkv, const __bf16* __restrict__ xb,
        const __bf16* __restrict__ whr, const __bf16* __restrict__ whi, char* __restrict__ ws) {
  extern __shared__ __attribute__((aligned(16))) char smem[];
  const int lane = threadIdx.x & 63;
  const int wave = __builtin_amdgcn_readfirstlane(threadIdx.x >> 6);
  const int bid = blockIdx.x;
  TSDECL TS(0); TS(1);
  const int xcd = bid & 7, idx = bid >> 3;
  const int rt = idx / 10, ci = idx - 10 * rt;
  const int ct = (xcd & 1) * 10 + ci;
  const int lim = (xcd >> 1) * 512 + 512;
  const int m0 = (xcd >> 1) * 512 + rt * 192 + wave * 48;
  const int rot = (ci + 3 * rt) % 7;
  const int c = lane & 15, g = lane >> 4;
  unsigned avoff[2 * K1_MT];
  a_offsets<K1_MT>(avoff, m0, DM * 2, lane, lim - 1);
  f32x4 acc[K1_MT][7];
#pragma unroll
  for (int m = 0; m < K1_MT; ++m)
#pragma unroll
    for (int n = 0; n < 7; ++n) acc[m][n] = f32x4{0.f, 0.f, 0.f, 0.f};

  unsigned sink;
  {
    const int t0 = m0 < lim ? m0 : lim - 16;
    const char* tp = ct < 12 ? ws + (size_t)(ct >> 2) * WS_K + ((4 * (ct & 3) + (lane & 3)) * 64 + (t0 >> 5)) * 2048
                             : ws + WS_B + (((t0 + (ct >= 16 ? SEQ : 0)) >> 5) * 4 + (ct & 3)) * 7168;
    touch(sink, tp);
  }
  char* tb = smem + wave * 6144;
  if (ct < 8) {
    gemm_core<K1CFG, K1_MT, 0, true>((const char*)xa, avoff, (const char*)wqkv + ct * 224, 3 * DM * 2, smem, wave, lane, rot, acc);
    TS(2);
    const bool isq = ct < 4;
    const float sc = isq ? 0.18898223650461362f * 1.4426950408889634f : 1.0f;
    char* dst = ws + (isq ? WS_Q : WS_K);
    const unsigned padlo = isq ? 0u : 0x3f803f80u, padhi = isq ? 0u : 0x00003f80u;
#pragma unroll
    for (int m = 0; m < K1_MT; ++m) {
      char* st = tb + m * 24576;
#pragma unroll
      for (int nt = 0; nt < 7; ++nt) {
        const int n4 = 4 * nt + g;
        const int hh = (n4 * 37) >> 8;
        const int q4 = n4 - 7 * hh;
        char* p = st + (hh * 4 + (q4 >> 1)) * 256 + c * 16 + (q4 & 1) * 8;
        *(u32x2*)p = u32x2{pack2(acc[m][nt][0] * sc, acc[m][nt][1] * sc), pack2(acc[m][nt][2] * sc, acc[m][nt][3] * sc)};
        if (q4 == 6) *(u32x2*)(p + 8) = u32x2{padlo, padhi};
      }
    }
#pragma unroll
    for (int m = 0; m < K1_MT; ++m) {
      const int t0 = m0 + 16 * m;
      if (t0 < lim) {
#pragma unroll
        for (int hh = 0; hh < 4; ++hh) {
          const u32x4 v = *(const u32x4*)(tb + m * 24576 + hh * 1024 + lane * 16);
          const int lo = isq ? (t0 & 16) * 64 + c * 16 : ((c >> 2) & 1) * 1024 + ((t0 & 16) / 2 + (c >> 3) * 4 + (c & 3)) * 16;
          st16<WT1>(dst + ((4 * (ct & 3) + hh) * 64 + (t0 >> 5)) * 2048 + g * 256 + lo, v);
        }
      }
    }
  } else if (ct < 12) {
    gemm_core<K1CFG, K1_MT, 0, false>((const char*)xa, avoff, (const char*)wqkv + ct * 224, 3 * DM * 2, smem, wave, lane, rot, acc);
    TS(2);
    char* dst = ws + WS_V;
#pragma unroll
    for (int m = 0; m < K1_MT; ++m) {
      char* st = tb + m * 24576;
#pragma unroll
      for (int nt = 0; nt < 7; ++nt) {
        const int nl = 16 * nt + c;
        const int hh = (nl * 147) >> 12;
        const int d = nl - 28 * hh;
        char* p = st + hh * 1024 + (d >> 4) * 512 + (g >> 1) * 256 + (d & 15) * 16 + (g & 1) * 8;
        *(u32x2*)p = u32x2{pack2(acc[m][nt][0], acc[m][nt][1]), pack2(acc[m][nt][2], acc[m][nt][3])};
      }
      if (lane < 32) {
        const unsigned o1 = (lane & 3) == 0 ? 0x3f803f80u : 0u;
        *(u32x4*)(st + (lane >> 3) * 1024 + 512 + ((lane >> 2) & 1) * 256 + (12 + (lane & 3)) * 16) = u32x4{o1, o1, o1, o1};
      }
    }
#pragma unroll
    for (int m = 0; m < K1_MT; ++m) {
      const int t0 = m0 + 16 * m;
      if (t0 < lim) {
#pragma unroll
        for (int hh = 0; hh < 4; ++hh) {
          const u32x4 v = *(const u32x4*)(tb + m * 24576 + hh * 1024 + lane * 16);
          st16<WT1>(dst + ((4 * (ct & 3) + hh) * 64 + (t0 >> 5)) * 2048 + (lane >> 5) * 1024 + (t0 & 16) * 32 + (lane & 31) * 16, v);
        }
      }
    }
  } else {
    const bool isb = ct >= 16;
    const int nq = ct & 3;
    const char* W = (const char*)(isb ? whi : whr) + (size_t)nq * 112 * (DM * 2);
    gemm_core<K1CFG, K1_MT, 1, false>((const char*)xb, avoff, W, DM * 2, smem, wave, lane, rot, acc);
    TS(2);
    const float sg = isb ? -1.f : 1.f;
    char* dst = ws + WS_B;
#pragma unroll
    for (int m = 0; m < K1_MT; ++m) {
      char* st = tb + m * 24576;
#pragma unroll
      for (int nt = 0; nt < 7; ++nt)
        *(u32x2*)(st + nt * 512 + (g >> 1) * 256 + c * 16 + (g & 1) * 8) =
            u32x2{pack2(acc[m][nt][0] * sg, acc[m][nt][1] * sg), pack2(acc[m][nt][2] * sg, acc[m][nt][3] * sg)};
    }
#pragma unroll
    for (int m = 0; m < K1_MT; ++m) {
      const int t0 = m0 + 16 * m;
      if (t0 < lim) {
        const int kk = t0 + (isb ? SEQ : 0);
        char* gp = dst + ((kk >> 5) * 4 + nq) * 7168 + (kk & 16) * 32 + (lane >> 5) * 1024 + (lane & 31) * 16;
#pragma unroll
        for (int i = 0; i < 4; ++i) {
          if (i < 3 || lane < 32) {
            const u32x4 v = *(const u32x4*)(tb + m * 24576 + i * 1024 + lane * 16);
            st16<WT1>(gp + i * 2048, v);
          }
        }
      }
    }
  }
  keep(sink);
  TSEND(ws, 0);
}

__device__ __forceinline__ float col_max(const f32x4& a, const f32x4& b) {
  float m = fmaxf(fmaxf(fmaxf(a[0], a[1]), fmaxf(a[2], a[3])), fmaxf(fmaxf(b[0], b[1]), fmaxf(b[2], b[3])));
  auto r = __builtin_amdgcn_permlane32_swap(__builtin_bit_cast(unsigned, m), __builtin_bit_cast(unsigned, m), false, false);
  m = fmaxf(__builtin_bit_cast(float, (unsigned)r[0]), __builtin_bit_cast(float, (unsigned)r[1]));
  auto t = __builtin_amdgcn_permlane16_swap(__builtin_bit_cast(unsigned, m), __builtin_bit_cast(unsigned, m), false, false);
  return fmaxf(__builtin_bit_cast(float, (unsigned)t[0]), __builtin_bit_cast(float, (unsigned)t[1]));
}
__device__ __forceinline__ unsigned pack2p(float a, float b) {
  unsigned r = pack2(a, b);
  asm volatile("" : "+v"(r));
  return r;
}
#define MFMA16(a, b, c) __builtin_amdgcn_mfma_f32_16x16x32_bf16(a, b, c, 0, 0, 0)

__device__ __forceinline__ void attn_role(const char* __restrict__ ws, char* __restrict__ wso, char* smem, int wg, int wave, int lane) {
  TSDECL TS(0);
  const int xcd = wg & 7, idx = wg >> 3;
  const int head = xcd * 2 + (idx >> 4), qb = idx & 15;
  const bf16x8* Kf = (const bf16x8*)(ws + WS_K) + (size_t)(head * 64 + wave * 16) * 128 + lane;
  const bf16x8* Vf = (const bf16x8*)(ws + WS_V) + (size_t)(head * 64 + wave * 16) * 128 + lane;
  const f32x4 zero4 = {0.f, 0.f, 0.f, 0.f};

  const char* Kb = (const char*)(ws + WS_K) + (size_t)(head * 64 + wave * 16) * 2048;
  const unsigned l16 = lane * 16;
  bf16x8 kf[3][2], vf[3][2];
  bf16x8 qf[8];
  auto loadkv = [&](auto T) {
    constexpr int t = T;
    const char* src = Kb + t * 2048;
    gload16<0>(kf[t % 3][0], l16, src);
    gload16<1024>(kf[t % 3][1], l16, src);
    gload16<0>(vf[t % 3][0], l16, src + (WS_V - WS_K));
    gload16<1024>(vf[t % 3][1], l16, src + (WS_V - WS_K));
  };
  unsigned sink;
  touch(sink, wso + WS_O + ((size_t)(qb * 8 + 2 * wave + (lane & 1)) * 16 + head) * 1024);
  loadkv(std::integral_constant<int, 0>{});
  {
    const char* Qb = (const char*)(ws + WS_Q) + (size_t)(head * 64 + qb * 4) * 2048;
    static_for<4>([&](auto QT) {
      gload16<0>(qf[2 * QT], l16, Qb + QT * 2048);
      gload16<1024>(qf[2 * QT + 1], l16, Qb + QT * 2048);
    });
  }
  loadkv(std::integral_constant<int, 1>{});
  asm volatile("s_waitcnt vmcnt(4)" ::: "memory");
  tie2(kf[0][0], kf[0][1]); tie2(vf[0][0], vf[0][1]);
#pragma unroll
  for (int j = 0; j < 4; ++j) tie2(qf[2 * j], qf[2 * j + 1]);
  keep(sink);
  const bool top = lane >= 48;
  float mref[8];
#pragma unroll
  for (int j = 0; j < 8; ++j) {
    const f32x4 sa = MFMA16(kf[0][0], qf[j], zero4), sb = MFMA16(kf[0][1], qf[j], zero4);
    const float nm = -(col_max(sa, sb) + 64.f);
    const __bf16 b0 = (__bf16)nm;
    mref[j] = -(float)b0;
    if (top) qf[j][4] = b0;
  }
  TS(1);
  f32x4 o[8][2];
#pragma unroll
  for (int j = 0; j < 8; ++j) { o[j][0] = zero4; o[j][1] = zero4; }

  {
#define SB() __builtin_amdgcn_sched_barrier(0)
    f32x4 s0[2], s1[2];
    u32x4 p[2];
    float ea[2], eb[2];
    s0[0] = MFMA16(kf[0][0], qf[0], zero4);
    s1[0] = MFMA16(kf[0][1], qf[0], zero4);
    SB();
    static_for<128>([&](auto I) {
      constexpr int i = I, kt = i >> 3, j = i & 7;
      constexpr int ni = i + 1, nkt = ni >> 3, nj = ni & 7;
      constexpr int pi = i - 1, pkt = pi >> 3, pj = pi & 7;
      if constexpr (j == 7 && ni < 128) {
        asm volatile("s_waitcnt vmcnt(%0)" ::"i"(kt + 2 < 16 ? 4 : 0) : "memory");
        tie2(kf[nkt % 3][0], kf[nkt % 3][1]); tie2(vf[nkt % 3][0], vf[nkt % 3][1]);
      }
      if constexpr (ni < 128) s0[ni & 1] = MFMA16(kf[nkt % 3][0], qf[nj], zero4);
      SB();
      ea[0] = __builtin_amdgcn_exp2f(s0[i & 1][0]); ea[1] = __builtin_amdgcn_exp2f(s0[i & 1][1]);
      SB();
      if constexpr (i > 0) p[pi & 1][3] = pack2p(eb[0], eb[1]);
      SB();
      if constexpr (ni < 128) s1[ni & 1] = MFMA16(kf[nkt % 3][1], qf[nj], zero4);
      SB();
      eb[0] = __builtin_amdgcn_exp2f(s0[i & 1][2]); eb[1] = __builtin_amdgcn_exp2f(s0[i & 1][3]);
      SB();
      p[i & 1][0] = pack2p(ea[0], ea[1]);
      if constexpr (j == 1 && kt + 2 < 16) loadkv(std::integral_constant<int, kt + 2>{});
      SB();
      if constexpr (i > 0) o[pj][0] = MFMA16(vf[pkt % 3][0], __builtin_bit_cast(bf16x8, p[pi & 1]), o[pj][0]);
      SB();
      ea[0] = __builtin_amdgcn_exp2f(s1[i & 1][0]); ea[1] = __builtin_amdgcn_exp2f(s1[i & 1][1]);
      SB();
      p[i & 1][1] = pack2p(eb[0], eb[1]);
      SB();
      if constexpr (i > 0) o[pj][1] = MFMA16(vf[pkt % 3][1], __builtin_bit_cast(bf16x8, p[pi & 1]), o[pj][1]);
      SB();
      eb[0] = __builtin_amdgcn_exp2f(s1[i & 1][2]); eb[1] = __builtin_amdgcn_exp2f(s1[i & 1][3]);
      SB();
      p[i & 1][2] = pack2p(ea[0], ea[1]);
      SB();
    });
    p[1][3] = pack2(eb[0], eb[1]);
    o[7][0] = MFMA16(vf[15 % 3][0], __builtin_bit_cast(bf16x8, p[1]), o[7][0]);
    o[7][1] = MFMA16(vf[15 % 3][1], __builtin_bit_cast(bf16x8, p[1]), o[7][1]);
  }

  TS(2);
  bool bad = false;
  {
    float am = 0.f;
#pragma unroll
    for (int j = 0; j < 8; ++j) {
      const float l = o[j][1][0];
      bad |= top && !(l >= 0x1p-80f && l <= 0x1p64f);
#pragma unroll
      for (int e = 0; e < 4; ++e) am += fabsf(o[j][0][e]) + fabsf(o[j][1][e]);
    }
    bad |= !(am <= 0x1p120f);
  }
  if (__any(bad)) {
#pragma unroll
    for (int j = 0; j < 8; ++j) {
      if (top) qf[j][4] = (__bf16)0.f;
      o[j][0] = zero4; o[j][1] = zero4;
      mref[j] = -INFINITY;
    }
#pragma unroll 1
    for (int kt = 0; kt < 16; ++kt) {
      const bf16x8 k0 = Kf[kt * 128], k1 = Kf[kt * 128 + 64], v0 = Vf[kt * 128], v1 = Vf[kt * 128 + 64];
#pragma unroll
      for (int j = 0; j < 8; ++j) {
        const f32x4 sa = MFMA16(k0, qf[j], zero4), sb = MFMA16(k1, qf[j], zero4);
        const float mn = fmaxf(mref[j], col_max(sa, sb));
        const float al = __builtin_amdgcn_exp2f(mref[j] - mn);
        mref[j] = mn;
        bf16x8 pp;
#pragma unroll
        for (int e = 0; e < 4; ++e) {
          pp[e] = (__bf16)__builtin_amdgcn_exp2f(sa[e] - mn);
          pp[4 + e] = (__bf16)__builtin_amdgcn_exp2f(sb[e] - mn);
          o[j][0][e] *= al; o[j][1][e] *= al;
        }
        o[j][0] = MFMA16(v0, pp, o[j][0]);
        o[j][1] = MFMA16(v1, pp, o[j][1]);
      }
    }
  }

  f32x4* part = (f32x4*)smem;
  float* mbuf = (float*)(smem + 65536);
#pragma unroll
  for (int j = 0; j < 8; ++j) {
    mbuf[(j * 4 + wave) * 64 + lane] = top ? mref[j] + __builtin_amdgcn_logf(o[j][1][0]) : mref[j];
    if ((j >> 1) != wave) {
      part[((j * 4 + wave) * 2 + 0) * 64 + lane] = o[j][0];
      part[((j * 4 + wave) * 2 + 1) * 64 + lane] = o[j][1];
    }
  }
  f32x4 mine[2][2];
#pragma unroll
  for (int jj = 0; jj < 2; ++jj)
#pragma unroll
    for (int dt = 0; dt < 2; ++dt)
      mine[jj][dt] = wave == 0 ? o[jj][dt] : (wave == 1 ? o[2 + jj][dt] : (wave == 2 ? o[4 + jj][dt] : o[6 + jj][dt]));
  __syncthreads();
  const int c = lane & 15, g = lane >> 4;
#pragma unroll
  for (int jj = 0; jj < 2; ++jj) {
    const int j = 2 * wave + jj;
    float mw[4];
#pragma unroll
    for (int w = 0; w < 4; ++w) mw[w] = mbuf[(j * 4 + w) * 64 + 48 + c];
    const float mm = fmaxf(fmaxf(mw[0], mw[1]), fmaxf(mw[2], mw[3]));
#pragma unroll
    for (int w = 0; w < 4; ++w) mw[w] = mbuf[(j * 4 + w) * 64 + c];
    f32x4 r[2] = {zero4, zero4};
#pragma unroll
    for (int w = 0; w < 4; ++w) {
      const float f = __builtin_amdgcn_exp2f(mw[w] - mm);
#pragma unroll
      for (int dt = 0; dt < 2; ++dt) {
        f32x4 t;
        if (w == wave) t = mine[jj][dt];
        else t = part[((j * 4 + w) * 2 + dt) * 64 + lane];
#pragma unroll
        for (int e = 0; e < 4; ++e) r[dt][e] += f * t[e];
      }
    }
    const float inv = 1.0f / __shfl(r[1][0], 48 + c);
    const unsigned x0 = pack2(r[0][0] * inv, r[0][1] * inv), x1 = pack2(r[0][2] * inv, r[0][3] * inv);
    unsigned y0 = pack2(r[1][0] * inv, r[1][1] * inv), y1 = pack2(r[1][2] * inv, r[1][3] * inv);
    if (top) { y0 = 0u; y1 = 0u; }
    auto sa = __builtin_amdgcn_permlane16_swap(x0, y0, false, false);
    auto sb = __builtin_amdgcn_permlane16_swap(x1, y1, false, false);
    char* op = wso + WS_O + ((size_t)(qb * 8 + j) * 16 + head) * 1024 + ((g & 1) * 2 + (g >> 1)) * 256 + c * 16;
    st16<WT2>(op, u32x4{(unsigned)sa[0], (unsigned)sb[0], (unsigned)sa[1], (unsigned)sb[1]});
  }
  TSEND(wso, 1);
}

__global__ void __launch_bounds__(256, 2)
k2_main(const __bf16* __restrict__ csr, const __bf16* __restrict__ csi, char* __restrict__ ws) {
  extern __shared__ __attribute__((aligned(16))) char smem[];
  const int lane = threadIdx.x & 63;
  const int wave = __builtin_amdgcn_readfirstlane(threadIdx.x >> 6);
  const int bid = blockIdx.x;
  if (bid < 256) {
    attn_role(ws, ws, smem, bid, wave, lane);
  } else {
    TSDECL TS(0); TS(1);
    const int wg = bid - 256;
    const int xcd = wg & 7, idx = wg >> 3;
    const int ks = xcd >> 1, mt = (xcd & 1) * 8 + (idx >> 2), nq = idx & 3;
    const int m0 = mt * 128 + wave * 32;
    unsigned avoff[4];
    a_offsets<2>(avoff, m0, SEQ * 2, lane, SEQ - 1);
    const char* A = (const char*)(ks < 2 ? csr : csi) + (ks & 1) * 2048;
    const char* B = ws + WS_B + ((size_t)(ks * 32) * 4 + nq) * 7168;
    f32x4 acc[2][7];
#pragma unroll
    for (int m = 0; m < 2; ++m)
#pragma unroll
      for (int n = 0; n < 7; ++n) acc[m][n] = f32x4{0.f, 0.f, 0.f, 0.f};
    unsigned sink;
    touch(sink, ws + WS_P + ((size_t)ks * SEQ + m0 + (lane & 31)) * (DM * 2) + nq * 224);
    gemm_core<K2CFG, 2, 2, true>(A, avoff, B, 4 * 7168, smem, wave, lane, K2ROT, acc);
    keep(sink);
    TS(2);
    store_rows<2>(acc, smem + wave * 4096, 16384, ws + WS_P + ((size_t)ks * SEQ + m0) * (DM * 2) + nq * 224, DM * 2, lane);
    TSEND(ws, 1);
  }
}

__global__ void __launch_bounds__(256, 1)
k3_final(const __bf16* __restrict__ wo, const char* __restrict__ ws, __bf16* __restrict__ out) {
  extern __shared__ __attribute__((aligned(16))) char smem[];
  const int lane = threadIdx.x & 63;
  const int wave = __builtin_amdgcn_readfirstlane(threadIdx.x >> 6);
  const int bid = blockIdx.x;
  TSDECL TS(0); TS(1); TS(2);
  const int xcd = bid & 7, idx = bid >> 3;
  if (idx < 16) {
    const int rt = xcd * 4 + (idx >> 2), nq = idx & 3;
    const int m0 = rt * 64 + wave * 16;
    const unsigned avoff[2] = {(unsigned)(lane * 16), 0u};
    f32x4 acc[1][7];
#pragma unroll
    for (int n = 0; n < 7; ++n) acc[0][n] = f32x4{0.f, 0.f, 0.f, 0.f};
    unsigned sink;
    touch(sink, (const char*)out + (size_t)(m0 + (lane & 15)) * (2 * DM * 2) + nq * 224);
    gemm_core<K3CFG, 1, 3, true>(ws + WS_O + (size_t)(m0 >> 4) * 16384, avoff, (const char*)wo + nq * 224, DM * 2, smem, wave, lane,
                                    K3ROT, acc);
    keep(sink);
    store_rows<1>(acc, smem + 16 * 7168 + wave * 4096, 0, (char*)out + (size_t)m0 * (2 * DM * 2) + nq * 224, 2 * DM * 2, lane);
  } else {
    const int u0 = (xcd * 14 + idx - 16) * 1024 + threadIdx.x;
    unsigned sink;
    touch(sink, (const char*)out + (size_t)((u0 + (lane & 3) * 256) / 56) * (2 * DM * 2) + DM * 2);
    u32x4 t[4][4];
#pragma unroll
    for (int n = 0; n < 4; ++n)
#pragma unroll
      for (int ks = 0; ks < 4; ++ks) t[n][ks] = *(const u32x4*)(ws + WS_P + (size_t)ks * (SEQ * DM * 2) + (size_t)(u0 + n * 256) * 16);
    asm volatile("s_waitcnt vmcnt(0)" ::: "memory");
    keep(sink);
#pragma unroll
    for (int n = 0; n < 4; ++n) {
      u32x4 r;
#pragma unroll
      for (int e = 0; e < 4; ++e) {
        float lo = 0.f, hi = 0.f;
#pragma unroll
        for (int ks = 0; ks < 4; ++ks) {
          lo += __builtin_bit_cast(float, t[n][ks][e] << 16);
          hi += __builtin_bit_cast(float, t[n][ks][e] & 0xffff0000u);
        }
        r[e] = pack2(lo, hi);
      }
      const int u = u0 + n * 256, row = u / 56, ch = u - row * 56;
      st16<WT3>((char*)out + (size_t)row * (2 * DM * 2) + DM * 2 + ch * 16, r);
    }
  }
  TSEND(ws, 2);
}

extern "C" void kernel_launch(void* const* d_in, const int* in_sizes, int n_in, void* d_out, int out_size,
                              void* d_ws, size_t ws_size, hipStream_t stream) {
  const __bf16* xa = (const __bf16*)d_in[0];
  const __bf16* wqkv = (const __bf16*)d_in[1];
  const __bf16* wo = (const __bf16*)d_in[2];
  const __bf16* xb = (const __bf16*)d_in[3];
  const __bf16* csr = (const __bf16*)d_in[4];
  const __bf16* csi = (const __bf16*)d_in[5];
  const __bf16* whr = (const __bf16*)d_in[6];
  const __bf16* whi = (const __bf16*)d_in[7];
  char* ws = (char*)d_ws;
  constexpr int L1 = 3 * 24576 + 5 * 7168, L2 = 81920, L3 = 16 * 7168 + 4 * 4096;
  (void)hipFuncSetAttribute((const void*)k1_proj, hipFuncAttributeMaxDynamicSharedMemorySize, L1);
  (void)hipFuncSetAttribute((const void*)k2_main, hipFuncAttributeMaxDynamicSharedMemorySize, L2);
  (void)hipFuncSetAttribute((const void*)k3_final, hipFuncAttributeMaxDynamicSharedMemorySize, L3);
  k1_proj<<<240, 256, L1, stream>>>(xa, wqkv, xb, whr, whi, ws);
  k2_main<<<512, 256, L2, stream>>>(csr, csi, ws);
  k3_final<<<240, 256, L3, stream>>>(wo, ws, (__bf16*)d_out);
}
```

```cpp
#include <hip/hip_runtime.h>
#include <hip/hip_bf16.h>
#include <stdint.h>
#include <utility>

typedef __bf16 bf16x8 __attribute__((ext_vector_type(8)));
typedef __bf16 bf16x4 __attribute__((ext_vector_type(4)));
typedef __bf16 bf16x2 __attribute__((ext_vector_type(2)));
typedef short s16x4 __attribute__((ext_vector_type(4)));
typedef float f32x4 __attribute__((ext_vector_type(4)));
typedef float f32x16 __attribute__((ext_vector_type(16)));
typedef unsigned u32x4 __attribute__((ext_vector_type(4)));
typedef unsigned u32x2 __attribute__((ext_vector_type(2)));
#define LDSP __attribute__((address_space(3)))

#define TSDECL
#define TS(i)
#define TSEND(wsb, kidx)

#define ATROT 0
#define GPRIO 0
#define K3ROT ((2 * nq + (idx >> 2)) & 7)
#define K3CFG 8, 4, 8
#define K2CFG 16, 3, 4
#define ABURST(mt) ((mt) == 2)
#define WT1 true
#define WT2 true
#define WT3 true
#define XSLOT 1
#define K2ROT 0
constexpr int SEQ = 2048, DM = 448, NH = 16, DH = 28;

constexpr size_t WS_Q = 0;
constexpr size_t WS_K = 2u << 20;
constexpr size_t WS_V = 4u << 20;
constexpr size_t WS_B = 6u << 20;
constexpr size_t WS_O = 10u << 20;
constexpr size_t WS_P = 12u << 20;

template <int... Is, class F>
__device__ __forceinline__ void static_for_impl(std::integer_sequence<int, Is...>, F&& f) {
  (f(std::integral_constant<int, Is>{}), ...);
}
template <int N, class F>
__device__ __forceinline__ void static_for(F&& f) {
  static_for_impl(std::make_integer_sequence<int, N>{}, static_cast<F&&>(f));
}

__device__ __forceinline__ unsigned pack2(float a, float b) {
  bf16x2 v; v[0] = (__bf16)a; v[1] = (__bf16)b;
  return __builtin_bit_cast(unsigned, v);
}

template <int NKB, int NSA, int NSB, int MT>
struct Sched {
  static constexpr int NS = 2 * NKB;
  static constexpr int VB = 2, VA = 2 * MT;
  static constexpr int PA = NSA >= NKB ? NKB : NSA - 1;
  static constexpr int PB = NSB >= NS ? NS : NSB - 1;
  static constexpr int PM = (PA > (PB + 1) / 2) ? PA : (PB + 1) / 2;
  static constexpr int wait(int s) {
    int pos = 0, endB = 0, endA = 0;
    for (int p = 0; p < PM; ++p) {
      if (p < PA) { pos += VA; if (p == s / 2) endA = pos; }
      if (2 * p < PB) { pos += VB; if (2 * p == s) endB = pos; }
      if (2 * p + 1 < PB) { pos += VB; if (2 * p + 1 == s) endB = pos; }
    }
    for (int it = 0; it < s; ++it) {
      if (it + PB < NS) { pos += VB; if (it + PB == s) endB = pos; }
      if (it / 2 + PA < NKB && !(ABURST(MT) && (it & 1))) { pos += ABURST(MT) ? 2 * MT : MT; if (it / 2 + PA == s / 2) endA = pos; }
    }
    int e = endB > endA ? endB : endA;
    return pos - e;
  }
};

template <bool NT, bool L48>
__device__ __forceinline__ void glds16(unsigned voff, const void* sbase, unsigned lds_dst) {
  unsigned keep;
#define GLDS_(pre, pol, post) \
  asm volatile("s_mov_b32 %0, m0\n\ts_mov_b32 m0, %3\n\t" pre "s_nop 0\n\tglobal_load_lds_dwordx4 %1, %2" pol "\n\t" post "s_mov_b32 m0, %0" \
               : "=&s"(keep) : "v"(voff), "s"(sbase), "s"(lds_dst) : "memory")
  if constexpr (L48) {
    if constexpr (NT) GLDS_("s_mov_b32 exec_hi, 0xffff\n\t", " nt", "s_mov_b32 exec_hi, -1\n\t");
    else GLDS_("s_mov_b32 exec_hi, 0xffff\n\t", "", "s_mov_b32 exec_hi, -1\n\t");
  } else {
    if constexpr (NT) GLDS_("", " nt", "");
    else GLDS_("", "", "");
  }
#undef GLDS_
}
#define NTA(bk) ((bk) == 2)
#define NTB(bk) false

template <bool WT>
__device__ __forceinline__ void st16(void* p, u32x4 v) {
  if constexpr (WT) asm volatile("global_store_dwordx4 %0, %1, off sc1\n\ts_nop 1" ::"v"(p), "v"(v) : "memory");
  else *(u32x4*)p = v;
}

template <int OFF>
__device__ __forceinline__ void gload16(bf16x8& dst, unsigned voff, const void* sbase) {
  asm volatile("global_load_dwordx4 %0, %1, %2 offset:%3" : "=v"(dst) : "v"(voff), "s"(sbase), "i"(OFF) : "memory");
}
__device__ __forceinline__ void tie2(bf16x8& x, bf16x8& y) { asm volatile("" : "+v"(x), "+v"(y)); }

template <int NKB, int NSA, int NSB, int MT, int BKIND, bool SWAP>
__device__ __forceinline__ void gemm_core(const char* Abase, const unsigned (&avoff)[2 * MT], const char* Bbase, int ldb2,
                                          char* smem, int wave, int lane, int rot, f32x4 (&acc)[MT][7]) {
  using SC = Sched<NKB, NSA, NSB, MT>;
  constexpr int NS = SC::NS, PA = SC::PA, PB = SC::PB;
  constexpr int ASLOT = MT * 8192;
  constexpr bool BTR = BKIND == 0 || BKIND == 3, ADIR = BKIND == 3;
  constexpr int BOFF = ADIR ? 0 : NSA * ASLOT;
  constexpr int NSLB = ADIR ? NS : (MT == 3 ? NSB + XSLOT : NSB);
  const unsigned lds0 = (unsigned)(uintptr_t)(LDSP char*)smem;
  unsigned bvoff[2];
  static_for<2>([&](auto P) {
    constexpr int p = P;
    int L = wave * 1792 + p * 1024 + lane * 16;
    if (p == 1 && lane >= 48) L = wave * 1792;
    if constexpr (BTR) {
      int pos = L / 224, ch = (L - pos * 224) >> 4;
      int k = (pos & 3) + 8 * ((pos >> 2) & 1) + 4 * ((pos >> 3) & 1) + (pos & 16);
      if constexpr (BKIND == 3) k = k < 28 ? k : 27;
      bvoff[p] = k * ldb2 + ch * 16;
    } else if constexpr (BKIND == 1) {
      int n = L >> 6, chp = (L >> 4) & 3;
      bvoff[p] = n * ldb2 + 16 * (chp ^ ((-(n >> 2)) & 3));
    } else {
      bvoff[p] = L;
    }
  });
  const int bstep = (BKIND == 0) ? 32 * ldb2 : (BKIND == 1 ? 64 : (BKIND == 2 ? ldb2 : 28 * ldb2));
  bf16x8 fd[ADIR ? NS : 1][MT];
  auto issueB1 = [&](auto S, auto P) {
    constexpr int s = S;
    int se = s + 2 * rot;
    se = se >= NS ? se - NS : se;
    const char* src = Bbase + (ptrdiff_t)se * bstep;
    const unsigned d = lds0 + BOFF + (s % NSLB) * 7168 + wave * 1792;
    if constexpr (P == 0) glds16<NTB(BKIND), false>(bvoff[0], src, d);
    else glds16<NTB(BKIND), true>(bvoff[1], src, d + 1024);
  };
  auto issueA1 = [&](auto KB, auto P) {
    constexpr int kb = KB;
    int ke = kb + rot;
    ke = ke >= NKB ? ke - NKB : ke;
    if constexpr (ADIR) {
      gload16<(P & 1) * 1024>(fd[2 * kb + (P & 1)][P >> 1], avoff[P & ~1], Abase + ke * 2048);
    } else {
      const char* src = Abase + ke * 128;
      const unsigned d = lds0 + (kb % NSA) * ASLOT + wave * (MT * 2048);
      glds16<NTA(BKIND), false>(avoff[P], src, d + P * 1024);
    }
  };
  auto issueB = [&](auto S) { static_for<2>([&](auto P) { issueB1(S, P); }); };
  auto issueA = [&](auto KB) { static_for<2 * MT>([&](auto P) { issueA1(KB, P); }); };
  auto stepload = [&](auto S, auto J) {
    constexpr int s = S, j = J;
    if constexpr (j < 2) {
      if constexpr (s + PB < NS) issueB1(std::integral_constant<int, s + PB>{}, J);
    } else {
      if constexpr (s / 2 + PA < NKB) {
        if constexpr (!ABURST(MT)) {
          issueA1(std::integral_constant<int, s / 2 + PA>{}, std::integral_constant<int, (s & 1) * MT + j - 2>{});
        } else if constexpr ((s & 1) == 0) {
          issueA1(std::integral_constant<int, s / 2 + PA>{}, std::integral_constant<int, 2 * (j - 2)>{});
          issueA1(std::integral_constant<int, s / 2 + PA>{}, std::integral_constant<int, 2 * (j - 2) + 1>{});
        }
      }
    }
  };
  static_for<SC::PM>([&](auto P) {
    constexpr int p = P;
    if constexpr (p < PA) issueA(P);
    if constexpr (2 * p < PB) issueB(std::integral_constant<int, 2 * p>{});
    if constexpr (2 * p + 1 < PB) issueB(std::integral_constant<int, 2 * p + 1>{});
  });
  const int c = lane & 15, g = lane >> 4;
  int rb;
  if constexpr (BTR) rb = ((c >> 2) + 4 * (g & 1) + 16 * (g >> 1)) * 224 + 8 * (c & 3);
  else if constexpr (BKIND == 1) rb = c * 64 + 16 * (g ^ ((-(c >> 2)) & 3));
  else rb = lane * 16;
  const int ra0 = wave * (MT * 2048) + c * 128 + 16 * (g ^ (c >> 1));
  const int ra1 = ra0 ^ 64;

  bf16x8 fa[2][MT], fb[2][7];
  auto sync = [&](auto S) {
    constexpr int s = S;
    if constexpr (NSLB > NSB) asm volatile("s_waitcnt vmcnt(%0)" ::"i"(SC::wait(s)) : "memory");
    else asm volatile("s_waitcnt vmcnt(%0) lgkmcnt(0)" ::"i"(SC::wait(s)) : "memory");
    __builtin_amdgcn_s_barrier();
    if constexpr (BKIND == 2 && s == 0 && GPRIO > 0) __builtin_amdgcn_s_setprio(1);
    if constexpr (BKIND == 2 && s == GPRIO && GPRIO > 0) __builtin_amdgcn_s_setprio(0);
    if constexpr (ADIR && (s & 1) == 0) static_for<MT>([&](auto M) { tie2(fd[s][M], fd[s + 1][M]); });
    __builtin_amdgcn_sched_barrier(0);
  };
  constexpr int NRA = ADIR ? 0 : MT;
  constexpr int NSIDE = NRA + 7 + 2 + MT;
  auto side = [&](auto S, auto K) {
    constexpr int s = S, k = K;
    if constexpr (k < NRA) {
      const char* sa = smem + ((s / 2) % NSA) * ASLOT + ((s & 1) ? ra1 : ra0);
      fa[s & 1][k] = *(const bf16x8*)(sa + k * 2048);
    } else if constexpr (k < NRA + 7) {
      constexpr int nt = k - NRA;
      const char* sb = smem + BOFF + (s % NSLB) * 7168 + rb;
      if constexpr (BTR) {
        s16x4 lo = __builtin_amdgcn_ds_read_tr16_b64_v4i16((LDSP s16x4*)(sb + 32 * nt));
        s16x4 hi = __builtin_amdgcn_ds_read_tr16_b64_v4i16((LDSP s16x4*)(sb + 8 * 224 + 32 * nt));
        typedef short s16x8 __attribute__((ext_vector_type(8)));
        s16x8 t = __builtin_shufflevector(lo, hi, 0, 1, 2, 3, 4, 5, 6, 7);
        fb[s & 1][nt] = __builtin_bit_cast(bf16x8, t);
      } else {
        fb[s & 1][nt] = *(const bf16x8*)(sb + nt * 1024);
      }
    } else {
      stepload(S, std::integral_constant<int, k - NRA - 7>{});
    }
    __builtin_amdgcn_sched_barrier(0);
  };
  sync(std::integral_constant<int, 0>{});
  static_for<NSIDE>([&](auto K) { side(std::integral_constant<int, 0>{}, K); });
  static_for<NS>([&](auto S) {
    constexpr int s = S;
    if constexpr (s + 1 < NS) sync(std::integral_constant<int, s + 1>{});
    static_for<7>([&](auto N) {
      constexpr int nt = N;
      static_for<MT>([&](auto M) {
        constexpr int m = M;
        const bf16x8 av = ADIR ? fd[ADIR ? s : 0][m] : fa[s & 1][m];
        if constexpr (SWAP)
          acc[m][nt] = __builtin_amdgcn_mfma_f32_16x16x32_bf16(fb[s & 1][nt], av, acc[m][nt], 0, 0, 0);
        else
          acc[m][nt] = __builtin_amdgcn_mfma_f32_16x16x32_bf16(av, fb[s & 1][nt], acc[m][nt], 0, 0, 0);
        __builtin_amdgcn_sched_barrier(0);
        if constexpr (s + 1 < NS) {
          static_for<NSIDE>([&](auto K) {
            if constexpr ((K * 7 * MT) / NSIDE == nt * MT + m) side(std::integral_constant<int, s + 1>{}, K);
          });
        }
      });
    });
  });
}

template <int MT>
__device__ __forceinline__ void a_offsets(unsigned (&avoff)[2 * MT], int m0, int lda2, int lane, int maxrow) {
#pragma unroll
  for (int p = 0; p < 2 * MT; ++p) {
    const int r = 8 * p + (lane >> 3);
    int row = m0 + r;
    row = row < maxrow ? row : maxrow;
    avoff[p] = row * lda2 + 16 * ((lane & 7) ^ ((r >> 1) & 7));
  }
}

template <int MT>
__device__ __forceinline__ void store_rows(const f32x4 (&acc)[MT][7], char* tb, int tstride, char* dst, int ld2, int lane) {
  const int c = lane & 15, g = lane >> 4;
#pragma unroll
  for (int m = 0; m < MT; ++m)
#pragma unroll
    for (int n = 0; n < 7; ++n)
      *(u32x2*)(tb + m * tstride + c * 240 + n * 32 + g * 8) = u32x2{pack2(acc[m][n][0], acc[m][n][1]), pack2(acc[m][n][2], acc[m][n][3])};
#pragma unroll
  for (int m = 0; m < MT; ++m)
#pragma unroll
    for (int i = 0; i < 4; ++i) {
      const int u = i * 64 + lane;
      const int row = (u * 4682) >> 16, ch = u - row * 14;
      if (i < 3 || lane < 32) {
        const u32x4 v = *(const u32x4*)(tb + m * tstride + row * 240 + ch * 16);
        st16<MT == 2 ? WT2 : WT3>(dst + (size_t)(m * 16 + row) * ld2 + ch * 16, v);
      }
    }
}

constexpr int K1_MT = 3;
#define K1CFG 7, 3, 4
__global__ void __launch_bounds__(256, 1)
k1_proj(const __bf16* __restrict__ xa, const __bf16* __restrict__ wqkv, const __bf16* __restrict__ xb,
        const __bf16* __restrict__ whr, const __bf16* __restrict__ whi, char* __restrict__ ws) {
  extern __shared__ __attribute__((aligned(16))) char smem[];
  const int lane = threadIdx.x & 63;
  const int wave = __builtin_amdgcn_readfirstlane(threadIdx.x >> 6);
  const int bid = blockIdx.x;
  TSDECL TS(0); TS(1);
  const int xcd = bid & 7, idx = bid >> 3;
  const int rt = idx / 10, ci = idx - 10 * rt;
  const int ct = (xcd & 1) * 10 + ci;
  const int lim = (xcd >> 1) * 512 + 512;
  const int m0 = (xcd >> 1) * 512 + rt * 192 + wave * 48;
  const int rot = (ci + 3 * rt) % 7;
  const int c = lane & 15, g = lane >> 4;
  unsigned avoff[2 * K1_MT];
  a_offsets<K1_MT>(avoff, m0, DM * 2, lane, lim - 1);
  f32x4 acc[K1_MT][7];
#pragma unroll
  for (int m = 0; m < K1_MT; ++m)
#pragma unroll
    for (int n = 0; n < 7; ++n) acc[m][n] = f32x4{0.f, 0.f, 0.f, 0.f};

  char* tb = smem + wave * 6144;
  if (ct < 8) {
    gemm_core<K1CFG, K1_MT, 0, true>((const char*)xa, avoff, (const char*)wqkv + ct * 224, 3 * DM * 2, smem, wave, lane, rot, acc);
    TS(2);
    const bool isq = ct < 4;
    const float sc = isq ? 0.18898223650461362f * 1.4426950408889634f : 1.0f;
    char* dst = ws + (isq ? WS_Q : WS_K);
    const unsigned padlo = isq ? 0u : 0x3f803f80u, padhi = isq ? 0u : 0x00003f80u;
#pragma unroll
    for (int m = 0; m < K1_MT; ++m) {
      char* st = tb + m * 24576;
#pragma unroll
      for (int nt = 0; nt < 7; ++nt) {
        const int n4 = 4 * nt + g;
        const int hh = (n4 * 37) >> 8;
        const int q4 = n4 - 7 * hh;
        char* p = st + (hh * 4 + (q4 >> 1)) * 256 + c * 16 + (q4 & 1) * 8;
        *(u32x2*)p = u32x2{pack2(acc[m][nt][0] * sc, acc[m][nt][1] * sc), pack2(acc[m][nt][2] * sc, acc[m][nt][3] * sc)};
        if (q4 == 6) *(u32x2*)(p + 8) = u32x2{padlo, padhi};
      }
    }
#pragma unroll
    for (int m = 0; m < K1_MT; ++m) {
      const int t0 = m0 + 16 * m;
      if (t0 < lim) {
#pragma unroll
        for (int hh = 0; hh < 4; ++hh) {
          const u32x4 v = *(const u32x4*)(tb + m * 24576 + hh * 1024 + lane * 16);
          const int lo = isq ? (t0 & 16) * 64 + c * 16 : ((c >> 2) & 1) * 1024 + ((t0 & 16) / 2 + (c >> 3) * 4 + (c & 3)) * 16;
          st16<WT1>(dst + ((4 * (ct & 3) + hh) * 64 + (t0 >> 5)) * 2048 + g * 256 + lo, v);
        }
      }
    }
  } else if (ct < 12) {
    gemm_core<K1CFG, K1_MT, 0, false>((const char*)xa, avoff, (const char*)wqkv + ct * 224, 3 * DM * 2, smem, wave, lane, rot, acc);
    TS(2);
    char* dst = ws + WS_V;
#pragma unroll
    for (int m = 0; m < K1_MT; ++m) {
      char* st = tb + m * 24576;
#pragma unroll
      for (int nt = 0; nt < 7; ++nt) {
        const int nl = 16 * nt + c;
        const int hh = (nl * 147) >> 12;
        const int d = nl - 28 * hh;
        char* p = st + hh * 1024 + (d >> 4) * 512 + (g >> 1) * 256 + (d & 15) * 16 + (g & 1) * 8;
        *(u32x2*)p = u32x2{pack2(acc[m][nt][0], acc[m][nt][1]), pack2(acc[m][nt][2], acc[m][nt][3])};
      }
      if (lane < 32) {
        const unsigned o1 = (lane & 3) == 0 ? 0x3f803f80u : 0u;
        *(u32x4*)(st + (lane >> 3) * 1024 + 512 + ((lane >> 2) & 1) * 256 + (12 + (lane & 3)) * 16) = u32x4{o1, o1, o1, o1};
      }
    }
#pragma unroll
    for (int m = 0; m < K1_MT; ++m) {
      const int t0 = m0 + 16 * m;
      if (t0 < lim) {
#pragma unroll
        for (int hh = 0; hh < 4; ++hh) {
          const u32x4 v = *(const u32x4*)(tb + m * 24576 + hh * 1024 + lane * 16);
          st16<WT1>(dst + ((4 * (ct & 3) + hh) * 64 + (t0 >> 5)) * 2048 + (lane >> 5) * 1024 + (t0 & 16) * 32 + (lane & 31) * 16, v);
        }
      }
    }
  } else {
    const bool isb = ct >= 16;
    const int nq = ct & 3;
    const char* W = (const char*)(isb ? whi : whr) + (size_t)nq * 112 * (DM * 2);
    gemm_core<K1CFG, K1_MT, 1, false>((const char*)xb, avoff, W, DM * 2, smem, wave, lane, rot, acc);
    TS(2);
    const float sg = isb ? -1.f : 1.f;
    char* dst = ws + WS_B;
#pragma unroll
    for (int m = 0; m < K1_MT; ++m) {
      char* st = tb + m * 24576;
#pragma unroll
      for (int nt = 0; nt < 7; ++nt)
        *(u32x2*)(st + nt * 512 + (g >> 1) * 256 + c * 16 + (g & 1) * 8) =
            u32x2{pack2(acc[m][nt][0] * sg, acc[m][nt][1] * sg), pack2(acc[m][nt][2] * sg, acc[m][nt][3] * sg)};
    }
#pragma unroll
    for (int m = 0; m < K1_MT; ++m) {
      const int t0 = m0 + 16 * m;
      if (t0 < lim) {
        const int kk = t0 + (isb ? SEQ : 0);
        char* gp = dst + ((kk >> 5) * 4 + nq) * 7168 + (kk & 16) * 32 + (lane >> 5) * 1024 + (lane & 31) * 16;
#pragma unroll
        for (int i = 0; i < 4; ++i) {
          if (i < 3 || lane < 32) {
            const u32x4 v = *(const u32x4*)(tb + m * 24576 + i * 1024 + lane * 16);
            st16<WT1>(gp + i * 2048, v);
          }
        }
      }
    }
  }
  TSEND(ws, 0);
}

__device__ __forceinline__ float col_max(const f32x4& a, const f32x4& b) {
  float m = fmaxf(fmaxf(fmaxf(a[0], a[1]), fmaxf(a[2], a[3])), fmaxf(fmaxf(b[0], b[1]), fmaxf(b[2], b[3])));
  auto r = __builtin_amdgcn_permlane32_swap(__builtin_bit_cast(unsigned, m), __builtin_bit_cast(unsigned, m), false, false);
  m = fmaxf(__builtin_bit_cast(float, (unsigned)r[0]), __builtin_bit_cast(float, (unsigned)r[1]));
  auto t = __builtin_amdgcn_permlane16_swap(__builtin_bit_cast(unsigned, m), __builtin_bit_cast(unsigned, m), false, false);
  return fmaxf(__builtin_bit_cast(float, (unsigned)t[0]), __builtin_bit_cast(float, (unsigned)t[1]));
}
__device__ __forceinline__ unsigned pack2p(float a, float b) {
  unsigned r = pack2(a, b);
  asm volatile("" : "+v"(r));
  return r;
}
#define MFMA16(a, b, c) __builtin_amdgcn_mfma_f32_16x16x32_bf16(a, b, c, 0, 0, 0)

__device__ __forceinline__ void attn_role(const char* __restrict__ ws, char* __restrict__ wso, char* smem, int wg, int wave, int lane) {
  TSDECL TS(0);
  const int xcd = wg & 7, idx = wg >> 3;
  const int head = xcd * 2 + (idx >> 4), qb = idx & 15;
  const bf16x8* Kf = (const bf16x8*)(ws + WS_K) + (size_t)(head * 64 + wave * 16) * 128 + lane;
  const bf16x8* Vf = (const bf16x8*)(ws + WS_V) + (size_t)(head * 64 + wave * 16) * 128 + lane;
  const f32x4 zero4 = {0.f, 0.f, 0.f, 0.f};

  const char* Kb = (const char*)(ws + WS_K) + (size_t)(head * 64 + wave * 16) * 2048;
  const unsigned l16 = lane * 16;
  bf16x8 kf[3][2], vf[3][2];
  bf16x8 qf[8];
  auto loadkv = [&](auto T) {
    constexpr int t = T;
    const char* src = Kb + t * 2048;
    gload16<0>(kf[t % 3][0], l16, src);
    gload16<1024>(kf[t % 3][1], l16, src);
    gload16<0>(vf[t % 3][0], l16, src + (WS_V - WS_K));
    gload16<1024>(vf[t % 3][1], l16, src + (WS_V - WS_K));
  };
  loadkv(std::integral_constant<int, 0>{});
  {
    const char* Qb = (const char*)(ws + WS_Q) + (size_t)(head * 64 + qb * 4) * 2048;
    static_for<4>([&](auto QT) {
      gload16<0>(qf[2 * QT], l16, Qb + QT * 2048);
      gload16<1024>(qf[2 * QT + 1], l16, Qb + QT * 2048);
    });
  }
  loadkv(std::integral_constant<int, 1>{});
  asm volatile("s_waitcnt vmcnt(4)" ::: "memory");
  tie2(kf[0][0], kf[0][1]); tie2(vf[0][0], vf[0][1]);
#pragma unroll
  for (int j = 0; j < 4; ++j) tie2(qf[2 * j], qf[2 * j + 1]);
  const bool top = lane >= 48;
  float mref[8];
#pragma unroll
  for (int j = 0; j < 8; ++j) {
    const f32x4 sa = MFMA16(kf[0][0], qf[j], zero4), sb = MFMA16(kf[0][1], qf[j], zero4);
    const float nm = -(col_max(sa, sb) + 64.f);
    const __bf16 b0 = (__bf16)nm;
    mref[j] = -(float)b0;
    if (top) qf[j][4] = b0;
  }
  TS(1);
  f32x4 o[8][2];
#pragma unroll
  for (int j = 0; j < 8; ++j) { o[j][0] = zero4; o[j][1] = zero4; }

  {
#define SB() __builtin_amdgcn_sched_barrier(0)
    f32x4 s0[2], s1[2];
    u32x4 p[2];
    float ea[2], eb[2];
    s0[0] = MFMA16(kf[0][0], qf[0], zero4);
    s1[0] = MFMA16(kf[0][1], qf[0], zero4);
    SB();
    static_for<128>([&](auto I) {
      constexpr int i = I, kt = i >> 3, j = i & 7;
      constexpr int ni = i + 1, nkt = ni >> 3, nj = ni & 7;
      constexpr int pi = i - 1, pkt = pi >> 3, pj = pi & 7;
      if constexpr (j == 7 && ni < 128) {
        asm volatile("s_waitcnt vmcnt(%0)" ::"i"(kt + 2 < 16 ? 4 : 0) : "memory");
        tie2(kf[nkt % 3][0], kf[nkt % 3][1]); tie2(vf[nkt % 3][0], vf[nkt % 3][1]);
      }
      if constexpr (ni < 128) s0[ni & 1] = MFMA16(kf[nkt % 3][0], qf[nj], zero4);
      SB();
      ea[0] = __builtin_amdgcn_exp2f(s0[i & 1][0]); ea[1] = __builtin_amdgcn_exp2f(s0[i & 1][1]);
      SB();
      if constexpr (i > 0) p[pi & 1][3] = pack2p(eb[0], eb[1]);
      SB();
      if constexpr (ni < 128) s1[ni & 1] = MFMA16(kf[nkt % 3][1], qf[nj], zero4);
      SB();
      eb[0] = __builtin_amdgcn_exp2f(s0[i & 1][2]); eb[1] = __builtin_amdgcn_exp2f(s0[i & 1][3]);
      SB();
      p[i & 1][0] = pack2p(ea[0], ea[1]);
      if constexpr (j == 1 && kt + 2 < 16) loadkv(std::integral_constant<int, kt + 2>{});
      SB();
      if constexpr (i > 0) o[pj][0] = MFMA16(vf[pkt % 3][0], __builtin_bit_cast(bf16x8, p[pi & 1]), o[pj][0]);
      SB();
      ea[0] = __builtin_amdgcn_exp2f(s1[i & 1][0]); ea[1] = __builtin_amdgcn_exp2f(s1[i & 1][1]);
      SB();
      p[i & 1][1] = pack2p(eb[0], eb[1]);
      SB();
      if constexpr (i > 0) o[pj][1] = MFMA16(vf[pkt % 3][1], __builtin_bit_cast(bf16x8, p[pi & 1]), o[pj][1]);
      SB();
      eb[0] = __builtin_amdgcn_exp2f(s1[i & 1][2]); eb[1] = __builtin_amdgcn_exp2f(s1[i & 1][3]);
      SB();
      p[i & 1][2] = pack2p(ea[0], ea[1]);
      SB();
    });
    p[1][3] = pack2(eb[0], eb[1]);
    o[7][0] = MFMA16(vf[15 % 3][0], __builtin_bit_cast(bf16x8, p[1]), o[7][0]);
    o[7][1] = MFMA16(vf[15 % 3][1], __builtin_bit_cast(bf16x8, p[1]), o[7][1]);
  }

  TS(2);
  bool bad = false;
  {
    float am = 0.f;
#pragma unroll
    for (int j = 0; j < 8; ++j) {
      const float l = o[j][1][0];
      bad |= top && !(l >= 0x1p-80f && l <= 0x1p64f);
#pragma unroll
      for (int e = 0; e < 4; ++e) am += fabsf(o[j][0][e]) + fabsf(o[j][1][e]);
    }
    bad |= !(am <= 0x1p120f);
  }
  if (__any(bad)) {
#pragma unroll
    for (int j = 0; j < 8; ++j) {
      if (top) qf[j][4] = (__bf16)0.f;
      o[j][0] = zero4; o[j][1] = zero4;
      mref[j] = -INFINITY;
    }
#pragma unroll 1
    for (int kt = 0; kt < 16; ++kt) {
      const bf16x8 k0 = Kf[kt * 128], k1 = Kf[kt * 128 + 64], v0 = Vf[kt * 128], v1 = Vf[kt * 128 + 64];
#pragma unroll
      for (int j = 0; j < 8; ++j) {
        const f32x4 sa = MFMA16(k0, qf[j], zero4), sb = MFMA16(k1, qf[j], zero4);
        const float mn = fmaxf(mref[j], col_max(sa, sb));
        const float al = __builtin_amdgcn_exp2f(mref[j] - mn);
        mref[j] = mn;
        bf16x8 pp;
#pragma unroll
        for (int e = 0; e < 4; ++e) {
          pp[e] = (__bf16)__builtin_amdgcn_exp2f(sa[e] - mn);
          pp[4 + e] = (__bf16)__builtin_amdgcn_exp2f(sb[e] - mn);
          o[j][0][e] *= al; o[j][1][e] *= al;
        }
        o[j][0] = MFMA16(v0, pp, o[j][0]);
        o[j][1] = MFMA16(v1, pp, o[j][1]);
      }
    }
  }

  f32x4* part = (f32x4*)smem;
  float* mbuf = (float*)(smem + 65536);
#pragma unroll
  for (int j = 0; j < 8; ++j) {
    mbuf[(j * 4 + wave) * 64 + lane] = top ? mref[j] + __builtin_amdgcn_logf(o[j][1][0]) : mref[j];
    if ((j >> 1) != wave) {
      part[((j * 4 + wave) * 2 + 0) * 64 + lane] = o[j][0];
      part[((j * 4 + wave) * 2 + 1) * 64 + lane] = o[j][1];
    }
  }
  f32x4 mine[2][2];
#pragma unroll
  for (int jj = 0; jj < 2; ++jj)
#pragma unroll
    for (int dt = 0; dt < 2; ++dt)
      mine[jj][dt] = wave == 0 ? o[jj][dt] : (wave == 1 ? o[2 + jj][dt] : (wave == 2 ? o[4 + jj][dt] : o[6 + jj][dt]));
  __syncthreads();
  const int c = lane & 15, g = lane >> 4;
#pragma unroll
  for (int jj = 0; jj < 2; ++jj) {
    const int j = 2 * wave + jj;
    float mw[4];
#pragma unroll
    for (int w = 0; w < 4; ++w) mw[w] = mbuf[(j * 4 + w) * 64 + 48 + c];
    const float mm = fmaxf(fmaxf(mw[0], mw[1]), fmaxf(mw[2], mw[3]));
#pragma unroll
    for (int w = 0; w < 4; ++w) mw[w] = mbuf[(j * 4 + w) * 64 + c];
    f32x4 r[2] = {zero4, zero4};
#pragma unroll
    for (int w = 0; w < 4; ++w) {
      const float f = __builtin_amdgcn_exp2f(mw[w] - mm);
#pragma unroll
      for (int dt = 0; dt < 2; ++dt) {
        f32x4 t;
        if (w == wave) t = mine[jj][dt];
        else t = part[((j * 4 + w) * 2 + dt) * 64 + lane];
#pragma unroll
        for (int e = 0; e < 4; ++e) r[dt][e] += f * t[e];
      }
    }
    const float inv = 1.0f / __shfl(r[1][0], 48 + c);
    const unsigned x0 = pack2(r[0][0] * inv, r[0][1] * inv), x1 = pack2(r[0][2] * inv, r[0][3] * inv);
    unsigned y0 = pack2(r[1][0] * inv, r[1][1] * inv), y1 = pack2(r[1][2] * inv, r[1][3] * inv);
    if (top) { y0 = 0u; y1 = 0u; }
    auto sa = __builtin_amdgcn_permlane16_swap(x0, y0, false, false);
    auto sb = __builtin_amdgcn_permlane16_swap(x1, y1, false, false);
    char* op = wso + WS_O + ((size_t)(qb * 8 + j) * 16 + head) * 1024 + ((g & 1) * 2 + (g >> 1)) * 256 + c * 16;
    st16<WT2>(op, u32x4{(unsigned)sa[0], (unsigned)sb[0], (unsigned)sa[1], (unsigned)sb[1]});
  }
  TSEND(wso, 1);
}

__global__ void __launch_bounds__(256, 2)
k2_main(const __bf16* __restrict__ csr, const __bf16* __restrict__ csi, char* __restrict__ ws) {
  extern __shared__ __attribute__((aligned(16))) char smem[];
  const int lane = threadIdx.x & 63;
  const int wave = __builtin_amdgcn_readfirstlane(threadIdx.x >> 6);
  const int bid = blockIdx.x;
  if (bid < 256) {
    attn_role(ws, ws, smem, bid, wave, lane);
  } else {
    TSDECL TS(0); TS(1);
    const int wg = bid - 256;
    const int xcd = wg & 7, idx = wg >> 3;
    const int ks = xcd >> 1, mt = (xcd & 1) * 8 + (idx >> 2), nq = idx & 3;
    const int m0 = mt * 128 + wave * 32;
    unsigned avoff[4];
    a_offsets<2>(avoff, m0, SEQ * 2, lane, SEQ - 1);
    const char* A = (const char*)(ks < 2 ? csr : csi) + (ks & 1) * 2048;
    const char* B = ws + WS_B + ((size_t)(ks * 32) * 4 + nq) * 7168;
    f32x4 acc[2][7];
#pragma unroll
    for (int m = 0; m < 2; ++m)
#pragma unroll
      for (int n = 0; n < 7; ++n) acc[m][n] = f32x4{0.f, 0.f, 0.f, 0.f};
    gemm_core<K2CFG, 2, 2, true>(A, avoff, B, 4 * 7168, smem, wave, lane, K2ROT, acc);
    TS(2);
    store_rows<2>(acc, smem + wave * 4096, 16384, ws + WS_P + ((size_t)ks * SEQ + m0) * (DM * 2) + nq * 224, DM * 2, lane);
    TSEND(ws, 1);
  }
}

__global__ void __launch_bounds__(256, 1)
k3_final(const __bf16* __restrict__ wo, const char* __restrict__ ws, __bf16* __restrict__ out) {
  extern __shared__ __attribute__((aligned(16))) char smem[];
  const int lane = threadIdx.x & 63;
  const int wave = __builtin_amdgcn_readfirstlane(threadIdx.x >> 6);
  const int bid = blockIdx.x;
  TSDECL TS(0); TS(1); TS(2);
  const int xcd = bid & 7, idx = bid >> 3;
  if (idx < 16) {
    const int rt = xcd * 4 + (idx >> 2), nq = idx & 3;
    const int m0 = rt * 64 + wave * 16;
    const unsigned avoff[2] = {(unsigned)(lane * 16), 0u};
    f32x4 acc[1][7];
#pragma unroll
    for (int n = 0; n < 7; ++n) acc[0][n] = f32x4{0.f, 0.f, 0.f, 0.f};
    gemm_core<K3CFG, 1, 3, true>(ws + WS_O + (size_t)(m0 >> 4) * 16384, avoff, (const char*)wo + nq * 224, DM * 2, smem, wave, lane,
                                    K3ROT, acc);
    store_rows<1>(acc, smem + 16 * 7168 + wave * 4096, 0, (char*)out + (size_t)m0 * (2 * DM * 2) + nq * 224, 2 * DM * 2, lane);
  } else {
    const int u0 = (xcd * 14 + idx - 16) * 1024 + threadIdx.x;
    u32x4 t[4][4];
#pragma unroll
    for (int n = 0; n < 4; ++n)
#pragma unroll
      for (int ks = 0; ks < 4; ++ks) t[n][ks] = *(const u32x4*)(ws + WS_P + (size_t)ks * (SEQ * DM * 2) + (size_t)(u0 + n * 256) * 16);
#pragma unroll
    for (int n = 0; n < 4; ++n) {
      u32x4 r;
#pragma unroll
      for (int e = 0; e < 4; ++e) {
        float lo = 0.f, hi = 0.f;
#pragma unroll
        for (int ks = 0; ks < 4; ++ks) {
          lo += __builtin_bit_cast(float, t[n][ks][e] << 16);
          hi += __builtin_bit_cast(float, t[n][ks][e] & 0xffff0000u);
        }
        r[e] = pack2(lo, hi);
      }
      const int u = u0 + n * 256, row = u / 56, ch = u - row * 56;
      st16<WT3>((char*)out + (size_t)row * (2 * DM * 2) + DM * 2 + ch * 16, r);
    }
  }
  TSEND(ws, 2);
}

extern "C" void kernel_launch(void* const* d_in, const int* in_sizes, int n_in, void* d_out, int out_size,
                              void* d_ws, size_t ws_size, hipStream_t stream) {
  const __bf16* xa = (const __bf16*)d_in[0];
  const __bf16* wqkv = (const __bf16*)d_in[1];
  const __bf16* wo = (const __bf16*)d_in[2];
  const __bf16* xb = (const __bf16*)d_in[3];
  const __bf16* csr = (const __bf16*)d_in[4];
  const __bf16* csi = (const __bf16*)d_in[5];
  const __bf16* whr = (const __bf16*)d_in[6];
  const __bf16* whi = (const __bf16*)d_in[7];
  char* ws = (char*)d_ws;
  constexpr int L1 = 3 * 24576 + 5 * 7168, L2 = 81920, L3 = 16 * 7168 + 4 * 4096;
  (void)hipFuncSetAttribute((const void*)k1_proj, hipFuncAttributeMaxDynamicSharedMemorySize, L1);
  (void)hipFuncSetAttribute((const void*)k2_main, hipFuncAttributeMaxDynamicSharedMemorySize, L2);
  (void)hipFuncSetAttribute((const void*)k3_final, hipFuncAttributeMaxDynamicSharedMemorySize, L3);
  k1_proj<<<240, 256, L1, stream>>>(xa, wqkv, xb, whr, whi, ws);
  k2_main<<<512, 256, L2, stream>>>(csr, csi, ws);
  k3_final<<<240, 256, L3, stream>>>(wo, ws, (__bf16*)d_out);
}
```

```cpp
#include <hip/hip_runtime.h>
#include <hip/hip_bf16.h>
#include <stdint.h>
#include <utility>

typedef __bf16 bf16x8 __attribute__((ext_vector_type(8)));
typedef __bf16 bf16x4 __attribute__((ext_vector_type(4)));
typedef __bf16 bf16x2 __attribute__((ext_vector_type(2)));
typedef short s16x4 __attribute__((ext_vector_type(4)));
typedef float f32x4 __attribute__((ext_vector_type(4)));
typedef float f32x16 __attribute__((ext_vector_type(16)));
typedef unsigned u32x4 __attribute__((ext_vector_type(4)));
typedef unsigned u32x2 __attribute__((ext_vector_type(2)));
#define LDSP __attribute__((address_space(3)))

#define TSDECL
#define TS(i)
#define TSEND(wsb, kidx)

#define ATROT 0
#define GPRIO 0
#define K3ROT ((2 * nq + (idx >> 2)) & 7)
#define K3CFG 8, 4, 8
#define K2CFG 16, 3, 4
#define ABURST(mt) ((mt) == 2)
#define WT1 true
#define WT2 true
#define WT3 true
#define XSLOT 1
#define K2ROT 0
constexpr int SEQ = 2048, DM = 448, NH = 16, DH = 28;

constexpr size_t WS_Q = 0;
constexpr size_t WS_K = 2u << 20;
constexpr size_t WS_V = 4u << 20;
constexpr size_t WS_B = 6u << 20;
constexpr size_t WS_O = 10u << 20;
constexpr size_t WS_P = 12u << 20;

template <int... Is, class F>
__device__ __forceinline__ void static_for_impl(std::integer_sequence<int, Is...>, F&& f) {
  (f(std::integral_constant<int, Is>{}), ...);
}
template <int N, class F>
__device__ __forceinline__ void static_for(F&& f) {
  static_for_impl(std::make_integer_sequence<int, N>{}, static_cast<F&&>(f));
}

__device__ __forceinline__ unsigned pack2(float a, float b) {
  bf16x2 v; v[0] = (__bf16)a; v[1] = (__bf16)b;
  return __builtin_bit_cast(unsigned, v);
}

template <int NKB, int NSA, int NSB, int MT>
struct Sched {
  static constexpr int NS = 2 * NKB;
  static constexpr int VB = 2, VA = 2 * MT;
  static constexpr int PA = NSA >= NKB ? NKB : NSA - 1;
  static constexpr int PB = NSB >= NS ? NS : NSB - 1;
  static constexpr int PM = (PA > (PB + 1) / 2) ? PA : (PB + 1) / 2;
  static constexpr int wait(int s) {
    int pos = 0, endB = 0, endA = 0;
    for (int p = 0; p < PM; ++p) {
      if (p < PA) { pos += VA; if (p == s / 2) endA = pos; }
      if (2 * p < PB) { pos += VB; if (2 * p == s) endB = pos; }
      if (2 * p + 1 < PB) { pos += VB; if (2 * p + 1 == s) endB = pos; }
    }
    for (int it = 0; it < s; ++it) {
      if (it + PB < NS) { pos += VB; if (it + PB == s) endB = pos; }
      if (it / 2 + PA < NKB && !(ABURST(MT) && (it & 1))) { pos += ABURST(MT) ? 2 * MT : MT; if (it / 2 + PA == s / 2) endA = pos; }
    }
    int e = endB > endA ? endB : endA;
    return pos - e;
  }
};

template <bool NT, bool L48>
__device__ __forceinline__ void glds16(unsigned voff, const void* sbase, unsigned lds_dst) {
  unsigned keep;
#define GLDS_(pre, pol, post) \
  asm volatile("s_mov_b32 %0, m0\n\ts_mov_b32 m0, %3\n\t" pre "s_nop 0\n\tglobal_load_lds_dwordx4 %1, %2" pol "\n\t" post "s_mov_b32 m0, %0" \
               : "=&s"(keep) : "v"(voff), "s"(sbase), "s"(lds_dst) : "memory")
  if constexpr (L48) {
    if constexpr (NT) GLDS_("s_mov_b32 exec_hi, 0xffff\n\t", " nt", "s_mov_b32 exec_hi, -1\n\t");
    else GLDS_("s_mov_b32 exec_hi, 0xffff\n\t", "", "s_mov_b32 exec_hi, -1\n\t");
  } else {
    if constexpr (NT) GLDS_("", " nt", "");
    else GLDS_("", "", "");
  }
#undef GLDS_
}
#define NTA(bk) ((bk) == 2)
#define NTB(bk) false

template <bool WT>
__device__ __forceinline__ void st16(void* p, u32x4 v) {
  if constexpr (WT) asm volatile("global_store_dwordx4 %0, %1, off sc1\n\ts_nop 1" ::"v"(p), "v"(v) : "memory");
  else *(u32x4*)p = v;
}

__device__ __forceinline__ void touch(unsigned& sink, const void* p) {
  asm volatile("global_load_dword %0, %1, off" : "=v"(sink) : "v"(p) : "memory");
}
__device__ __forceinline__ void keep(unsigned& sink) { asm volatile("s_waitcnt vmcnt(0)" : "+v"(sink)::"memory"); }

template <int OFF>
__device__ __forceinline__ void gload16(bf16x8& dst, unsigned voff, const void* sbase) {
  asm volatile("global_load_dwordx4 %0, %1, %2 offset:%3" : "=v"(dst) : "v"(voff), "s"(sbase), "i"(OFF) : "memory");
}
__device__ __forceinline__ void tie2(bf16x8& x, bf16x8& y) { asm volatile("" : "+v"(x), "+v"(y)); }

template <int NKB, int NSA, int NSB, int MT, int BKIND, bool SWAP>
__device__ __forceinline__ void gemm_core(const char* Abase, const unsigned (&avoff)[2 * MT], const char* Bbase, int ldb2,
                                          char* smem, int wave, int lane, int rot, f32x4 (&acc)[MT][7]) {
  using SC = Sched<NKB, NSA, NSB, MT>;
  constexpr int NS = SC::NS, PA = SC::PA, PB = SC::PB;
  constexpr int ASLOT = MT * 8192;
  constexpr bool BTR = BKIND == 0 || BKIND == 3, ADIR = BKIND == 3;
  constexpr int BOFF = ADIR ? 0 : NSA * ASLOT;
  constexpr int NSLB = ADIR ? NS : (MT == 3 ? NSB + XSLOT : NSB);
  const unsigned lds0 = (unsigned)(uintptr_t)(LDSP char*)smem;
  unsigned bvoff[2];
  static_for<2>([&](auto P) {
    constexpr int p = P;
    int L = wave * 1792 + p * 1024 + lane * 16;
    if (p == 1 && lane >= 48) L = wave * 1792;
    if constexpr (BTR) {
      int pos = L / 224, ch = (L - pos * 224) >> 4;
      int k = (pos & 3) + 8 * ((pos >> 2) & 1) + 4 * ((pos >> 3) & 1) + (pos & 16);
      if constexpr (BKIND == 3) k = k < 28 ? k : 27;
      bvoff[p] = k * ldb2 + ch * 16;
    } else if constexpr (BKIND == 1) {
      int n = L >> 6, chp = (L >> 4) & 3;
      bvoff[p] = n * ldb2 + 16 * (chp ^ ((-(n >> 2)) & 3));
    } else {
      bvoff[p] = L;
    }
  });
  const int bstep = (BKIND == 0) ? 32 * ldb2 : (BKIND == 1 ? 64 : (BKIND == 2 ? ldb2 : 28 * ldb2));
  bf16x8 fd[ADIR ? NS : 1][MT];
  auto issueB1 = [&](auto S, auto P) {
    constexpr int s = S;
    int se = s + 2 * rot;
    se = se >= NS ? se - NS : se;
    const char* src = Bbase + (ptrdiff_t)se * bstep;
    const unsigned d = lds0 + BOFF + (s % NSLB) * 7168 + wave * 1792;
    if constexpr (P == 0) glds16<NTB(BKIND), false>(bvoff[0], src, d);
    else glds16<NTB(BKIND), true>(bvoff[1], src, d + 1024);
  };
  auto issueA1 = [&](auto KB, auto P) {
    constexpr int kb = KB;
    int ke = kb + rot;
    ke = ke >= NKB ? ke - NKB : ke;
    if constexpr (ADIR) {
      gload16<(P & 1) * 1024>(fd[2 * kb + (P & 1)][P >> 1], avoff[P & ~1], Abase + ke * 2048);
    } else {
      const char* src = Abase + ke * 128;
      const unsigned d = lds0 + (kb % NSA) * ASLOT + wave * (MT * 2048);
      glds16<NTA(BKIND), false>(avoff[P], src, d + P * 1024);
    }
  };
  auto issueB = [&](auto S) { static_for<2>([&](auto P) { issueB1(S, P); }); };
  auto issueA = [&](auto KB) { static_for<2 * MT>([&](auto P) { issueA1(KB, P); }); };
  auto stepload = [&](auto S, auto J) {
    constexpr int s = S, j = J;
    if constexpr (j < 2) {
      if constexpr (s + PB < NS) issueB1(std::integral_constant<int, s + PB>{}, J);
    } else {
      if constexpr (s / 2 + PA < NKB) {
        if constexpr (!ABURST(MT)) {
          issueA1(std::integral_constant<int, s / 2 + PA>{}, std::integral_constant<int, (s & 1) * MT + j - 2>{});
        } else if constexpr ((s & 1) == 0) {
          issueA1(std::integral_constant<int, s / 2 + PA>{}, std::integral_constant<int, 2 * (j - 2)>{});
          issueA1(std::integral_constant<int, s / 2 + PA>{}, std::integral_constant<int, 2 * (j - 2) + 1>{});
        }
      }
    }
  };
  static_for<SC::PM>([&](auto P) {
    constexpr int p = P;
    if constexpr (p < PA) issueA(P);
    if constexpr (2 * p < PB) issueB(std::integral_constant<int, 2 * p>{});
    if constexpr (2 * p + 1 < PB) issueB(std::integral_constant<int, 2 * p + 1>{});
  });
  const int c = lane & 15, g = lane >> 4;
  int rb;
  if constexpr (BTR) rb = ((c >> 2) + 4 * (g & 1) + 16 * (g >> 1)) * 224 + 8 * (c & 3);
  else if constexpr (BKIND == 1) rb = c * 64 + 16 * (g ^ ((-(c >> 2)) & 3));
  else rb = lane * 16;
  const int ra0 = wave * (MT * 2048) + c * 128 + 16 * (g ^ (c >> 1));
  const int ra1 = ra0 ^ 64;

  bf16x8 fa[2][MT], fb[2][7];
  auto sync = [&](auto S) {
    constexpr int s = S;
    if constexpr (NSLB > NSB) asm volatile("s_waitcnt vmcnt(%0)" ::"i"(SC::wait(s)) : "memory");
    else asm volatile("s_waitcnt vmcnt(%0) lgkmcnt(0)" ::"i"(SC::wait(s)) : "memory");
    __builtin_amdgcn_s_barrier();
    if constexpr (BKIND == 2 && s == 0 && GPRIO > 0) __builtin_amdgcn_s_setprio(1);
    if constexpr (BKIND == 2 && s == GPRIO && GPRIO > 0) __builtin_amdgcn_s_setprio(0);
    if constexpr (ADIR && (s & 1) == 0) static_for<MT>([&](auto M) { tie2(fd[s][M], fd[s + 1][M]); });
    __builtin_amdgcn_sched_barrier(0);
  };
  constexpr int NRA = ADIR ? 0 : MT;
  constexpr int NSIDE = NRA + 7 + 2 + MT;
  auto side = [&](auto S, auto K) {
    constexpr int s = S, k = K;
    if constexpr (k < NRA) {
      const char* sa = smem + ((s / 2) % NSA) * ASLOT + ((s & 1) ? ra1 : ra0);
      fa[s & 1][k] = *(const bf16x8*)(sa + k * 2048);
    } else if constexpr (k < NRA + 7) {
      constexpr int nt = k - NRA;
      const char* sb = smem + BOFF + (s % NSLB) * 7168 + rb;
      if constexpr (BTR) {
        s16x4 lo = __builtin_amdgcn_ds_read_tr16_b64_v4i16((LDSP s16x4*)(sb + 32 * nt));
        s16x4 hi = __builtin_amdgcn_ds_read_tr16_b64_v4i16((LDSP s16x4*)(sb + 8 * 224 + 32 * nt));
        typedef short s16x8 __attribute__((ext_vector_type(8)));
        s16x8 t = __builtin_shufflevector(lo, hi, 0, 1, 2, 3, 4, 5, 6, 7);
        fb[s & 1][nt] = __builtin_bit_cast(bf16x8, t);
      } else {
        fb[s & 1][nt] = *(const bf16x8*)(sb + nt * 1024);
      }
    } else {
      stepload(S, std::integral_constant<int, k - NRA - 7>{});
    }
    __builtin_amdgcn_sched_barrier(0);
  };
  sync(std::integral_constant<int, 0>{});
  static_for<NSIDE>([&](auto K) { side(std::integral_constant<int, 0>{}, K); });
  static_for<NS>([&](auto S) {
    constexpr int s = S;
    if constexpr (s + 1 < NS) sync(std::integral_constant<int, s + 1>{});
    static_for<7>([&](auto N) {
      constexpr int nt = N;
      static_for<MT>([&](auto M) {
        constexpr int m = M;
        const bf16x8 av = ADIR ? fd[ADIR ? s : 0][m] : fa[s & 1][m];
        if constexpr (SWAP)
          acc[m][nt] = __builtin_amdgcn_mfma_f32_16x16x32_bf16(fb[s & 1][nt], av, acc[m][nt], 0, 0, 0);
        else
          acc[m][nt] = __builtin_amdgcn_mfma_f32_16x16x32_bf16(av, fb[s & 1][nt], acc[m][nt], 0, 0, 0);
        __builtin_amdgcn_sched_barrier(0);
        if constexpr (s + 1 < NS) {
          static_for<NSIDE>([&](auto K) {
            if constexpr ((K * 7 * MT) / NSIDE == nt * MT + m) side(std::integral_constant<int, s + 1>{}, K);
          });
        }
      });
    });
  });
}

template <int MT>
__device__ __forceinline__ void a_offsets(unsigned (&avoff)[2 * MT], int m0, int lda2, int lane, int maxrow) {
#pragma unroll
  for (int p = 0; p < 2 * MT; ++p) {
    const int r = 8 * p + (lane >> 3);
    int row = m0 + r;
    row = row < maxrow ? row : maxrow;
    avoff[p] = row * lda2 + 16 * ((lane & 7) ^ ((r >> 1) & 7));
  }
}

template <int MT>
__device__ __forceinline__ void store_rows(const f32x4 (&acc)[MT][7], char* tb, int tstride, char* dst, int ld2, int lane) {
  const int c = lane & 15, g = lane >> 4;
#pragma unroll
  for (int m = 0; m < MT; ++m)
#pragma unroll
    for (int n = 0; n < 7; ++n)
      *(u32x2*)(tb + m * tstride + c * 240 + n * 32 + g * 8) = u32x2{pack2(acc[m][n][0], acc[m][n][1]), pack2(acc[m][n][2], acc[m][n][3])};
#pragma unroll
  for (int m = 0; m < MT; ++m)
#pragma unroll
    for (int i = 0; i < 4; ++i) {
      const int u = i * 64 + lane;
      const int row = (u * 4682) >> 16, ch = u - row * 14;
      if (i < 3 || lane < 32) {
        const u32x4 v = *(const u32x4*)(tb + m * tstride + row * 240 + ch * 16);
        st16<MT == 2 ? WT2 : WT3>(dst + (size_t)(m * 16 + row) * ld2 + ch * 16, v);
      }
    }
}

constexpr int K1_MT = 3;
#define K1CFG 7, 3, 4
__global__ void __launch_bounds__(256, 1)
k1_proj(const __bf16* __restrict__ xa, const __bf16* __restrict__ wqkv, const __bf16* __restrict__ xb,
        const __bf16* __restrict__ whr, const __bf16* __restrict__ whi, char* __restrict__ ws) {
  extern __shared__ __attribute__((aligned(16))) char smem[];
  const int lane = threadIdx.x & 63;
  const int wave = __builtin_amdgcn_readfirstlane(threadIdx.x >> 6);
  const int bid = blockIdx.x;
  TSDECL TS(0); TS(1);
  const int xcd = bid & 7, idx = bid >> 3;
  const int rt = idx / 10, ci = idx - 10 * rt;
  const int ct = (xcd & 1) * 10 + ci;
  const int lim = (xcd >> 1) * 512 + 512;
  const int m0 = (xcd >> 1) * 512 + rt * 192 + wave * 48;
  const int rot = (ci + 3 * rt) % 7;
  const int c = lane & 15, g = lane >> 4;
  unsigned avoff[2 * K1_MT];
  a_offsets<K1_MT>(avoff, m0, DM * 2, lane, lim - 1);
  f32x4 acc[K1_MT][7];
#pragma unroll
  for (int m = 0; m < K1_MT; ++m)
#pragma unroll
    for (int n = 0; n < 7; ++n) acc[m][n] = f32x4{0.f, 0.f, 0.f, 0.f};

  char* tb = smem + wave * 6144;
  if (ct < 8) {
    gemm_core<K1CFG, K1_MT, 0, true>((const char*)xa, avoff, (const char*)wqkv + ct * 224, 3 * DM * 2, smem, wave, lane, rot, acc);
    TS(2);
    const bool isq = ct < 4;
    const float sc = isq ? 0.18898223650461362f * 1.4426950408889634f : 1.0f;
    char* dst = ws + (isq ? WS_Q : WS_K);
    const unsigned padlo = isq ? 0u : 0x3f803f80u, padhi = isq ? 0u : 0x00003f80u;
#pragma unroll
    for (int m = 0; m < K1_MT; ++m) {
      char* st = tb + m * 24576;
#pragma unroll
      for (int nt = 0; nt < 7; ++nt) {
        const int n4 = 4 * nt + g;
        const int hh = (n4 * 37) >> 8;
        const int q4 = n4 - 7 * hh;
        char* p = st + (hh * 4 + (q4 >> 1)) * 256 + c * 16 + (q4 & 1) * 8;
        *(u32x2*)p = u32x2{pack2(acc[m][nt][0] * sc, acc[m][nt][1] * sc), pack2(acc[m][nt][2] * sc, acc[m][nt][3] * sc)};
        if (q4 == 6) *(u32x2*)(p + 8) = u32x2{padlo, padhi};
      }
    }
#pragma unroll
    for (int m = 0; m < K1_MT; ++m) {
      const int t0 = m0 + 16 * m;
      if (t0 < lim) {
#pragma unroll
        for (int hh = 0; hh < 4; ++hh) {
          const u32x4 v = *(const u32x4*)(tb + m * 24576 + hh * 1024 + lane * 16);
          const int lo = isq ? (t0 & 16) * 64 + c * 16 : ((c >> 2) & 1) * 1024 + ((t0 & 16) / 2 + (c >> 3) * 4 + (c & 3)) * 16;
          st16<WT1>(dst + ((4 * (ct & 3) + hh) * 64 + (t0 >> 5)) * 2048 + g * 256 + lo, v);
        }
      }
    }
  } else if (ct < 12) {
    gemm_core<K1CFG, K1_MT, 0, false>((const char*)xa, avoff, (const char*)wqkv + ct * 224, 3 * DM * 2, smem, wave, lane, rot, acc);
    TS(2);
    char* dst = ws + WS_V;
#pragma unroll
    for (int m = 0; m < K1_MT; ++m) {
      char* st = tb + m * 24576;
#pragma unroll
      for (int nt = 0; nt < 7; ++nt) {
        const int nl = 16 * nt + c;
        const int hh = (nl * 147) >> 12;
        const int d = nl - 28 * hh;
        char* p = st + hh * 1024 + (d >> 4) * 512 + (g >> 1) * 256 + (d & 15) * 16 + (g & 1) * 8;
        *(u32x2*)p = u32x2{pack2(acc[m][nt][0], acc[m][nt][1]), pack2(acc[m][nt][2], acc[m][nt][3])};
      }
      if (lane < 32) {
        const unsigned o1 = (lane & 3) == 0 ? 0x3f803f80u : 0u;
        *(u32x4*)(st + (lane >> 3) * 1024 + 512 + ((lane >> 2) & 1) * 256 + (12 + (lane & 3)) * 16) = u32x4{o1, o1, o1, o1};
      }
    }
#pragma unroll
    for (int m = 0; m < K1_MT; ++m) {
      const int t0 = m0 + 16 * m;
      if (t0 < lim) {
#pragma unroll
        for (int hh = 0; hh < 4; ++hh) {
          const u32x4 v = *(const u32x4*)(tb + m * 24576 + hh * 1024 + lane * 16);
          st16<WT1>(dst + ((4 * (ct & 3) + hh) * 64 + (t0 >> 5)) * 2048 + (lane >> 5) * 1024 + (t0 & 16) * 32 + (lane & 31) * 16, v);
        }
      }
    }
  } else {
    const bool isb = ct >= 16;
    const int nq = ct & 3;
    const char* W = (const char*)(isb ? whi : whr) + (size_t)nq * 112 * (DM * 2);
    gemm_core<K1CFG, K1_MT, 1, false>((const char*)xb, avoff, W, DM * 2, smem, wave, lane, rot, acc);
    TS(2);
    const float sg = isb ? -1.f : 1.f;
    char* dst = ws + WS_B;
#pragma unroll
    for (int m = 0; m < K1_MT; ++m) {
      char* st = tb + m * 24576;
#pragma unroll
      for (int nt = 0; nt < 7; ++nt)
        *(u32x2*)(st + nt * 512 + (g >> 1) * 256 + c * 16 + (g & 1) * 8) =
            u32x2{pack2(acc[m][nt][0] * sg, acc[m][nt][1] * sg), pack2(acc[m][nt][2] * sg, acc[m][nt][3] * sg)};
    }
#pragma unroll
    for (int m = 0; m < K1_MT; ++m) {
      const int t0 = m0 + 16 * m;
      if (t0 < lim) {
        const int kk = t0 + (isb ? SEQ : 0);
        char* gp = dst + ((kk >> 5) * 4 + nq) * 7168 + (kk & 16) * 32 + (lane >> 5) * 1024 + (lane & 31) * 16;
#pragma unroll
        for (int i = 0; i < 4; ++i) {
          if (i < 3 || lane < 32) {
            const u32x4 v = *(const u32x4*)(tb + m * 24576 + i * 1024 + lane * 16);
            st16<WT1>(gp + i * 2048, v);
          }
        }
      }
    }
  }
  TSEND(ws, 0);
}

__device__ __forceinline__ float col_max(const f32x4& a, const f32x4& b) {
  float m = fmaxf(fmaxf(fmaxf(a[0], a[1]), fmaxf(a[2], a[3])), fmaxf(fmaxf(b[0], b[1]), fmaxf(b[2], b[3])));
  auto r = __builtin_amdgcn_permlane32_swap(__builtin_bit_cast(unsigned, m), __builtin_bit_cast(unsigned, m), false, false);
  m = fmaxf(__builtin_bit_cast(float, (unsigned)r[0]), __builtin_bit_cast(float, (unsigned)r[1]));
  auto t = __builtin_amdgcn_permlane16_swap(__builtin_bit_cast(unsigned, m), __builtin_bit_cast(unsigned, m), false, false);
  return fmaxf(__builtin_bit_cast(float, (unsigned)t[0]), __builtin_bit_cast(float, (unsigned)t[1]));
}
__device__ __forceinline__ unsigned pack2p(float a, float b) {
  unsigned r = pack2(a, b);
  asm volatile("" : "+v"(r));
  return r;
}
#define MFMA16(a, b, c) __builtin_amdgcn_mfma_f32_16x16x32_bf16(a, b, c, 0, 0, 0)

__device__ __forceinline__ void attn_role(const char* __restrict__ ws, char* __restrict__ wso, char* smem, int wg, int wave, int lane) {
  TSDECL TS(0);
  const int xcd = wg & 7, idx = wg >> 3;
  const int head = xcd * 2 + (idx >> 4), qb = idx & 15;
  const bf16x8* Kf = (const bf16x8*)(ws + WS_K) + (size_t)(head * 64 + wave * 16) * 128 + lane;
  const bf16x8* Vf = (const bf16x8*)(ws + WS_V) + (size_t)(head * 64 + wave * 16) * 128 + lane;
  const f32x4 zero4 = {0.f, 0.f, 0.f, 0.f};

  const char* Kb = (const char*)(ws + WS_K) + (size_t)(head * 64 + wave * 16) * 2048;
  const unsigned l16 = lane * 16;
  bf16x8 kf[3][2], vf[3][2];
  bf16x8 qf[8];
  auto loadkv = [&](auto T) {
    constexpr int t = T;
    const char* src = Kb + t * 2048;
    gload16<0>(kf[t % 3][0], l16, src);
    gload16<1024>(kf[t % 3][1], l16, src);
    gload16<0>(vf[t % 3][0], l16, src + (WS_V - WS_K));
    gload16<1024>(vf[t % 3][1], l16, src + (WS_V - WS_K));
  };
  loadkv(std::integral_constant<int, 0>{});
  {
    const char* Qb = (const char*)(ws + WS_Q) + (size_t)(head * 64 + qb * 4) * 2048;
    static_for<4>([&](auto QT) {
      gload16<0>(qf[2 * QT], l16, Qb + QT * 2048);
      gload16<1024>(qf[2 * QT + 1], l16, Qb + QT * 2048);
    });
  }
  loadkv(std::integral_constant<int, 1>{});
  asm volatile("s_waitcnt vmcnt(4)" ::: "memory");
  tie2(kf[0][0], kf[0][1]); tie2(vf[0][0], vf[0][1]);
#pragma unroll
  for (int j = 0; j < 4; ++j) tie2(qf[2 * j], qf[2 * j + 1]);
  const bool top = lane >= 48;
  float mref[8];
#pragma unroll
  for (int j = 0; j < 8; ++j) {
    const f32x4 sa = MFMA16(kf[0][0], qf[j], zero4), sb = MFMA16(kf[0][1], qf[j], zero4);
    const float nm = -(col_max(sa, sb) + 64.f);
    const __bf16 b0 = (__bf16)nm;
    mref[j] = -(float)b0;
    if (top) qf[j][4] = b0;
  }
  TS(1);
  f32x4 o[8][2];
#pragma unroll
  for (int j = 0; j < 8; ++j) { o[j][0] = zero4; o[j][1] = zero4; }

  {
#define SB() __builtin_amdgcn_sched_barrier(0)
    f32x4 s0[2], s1[2];
    u32x4 p[2];
    float ea[2], eb[2];
    s0[0] = MFMA16(kf[0][0], qf[0], zero4);
    s1[0] = MFMA16(kf[0][1], qf[0], zero4);
    SB();
    static_for<128>([&](auto I) {
      constexpr int i = I, kt = i >> 3, j = i & 7;
      constexpr int ni = i + 1, nkt = ni >> 3, nj = ni & 7;
      constexpr int pi = i - 1, pkt = pi >> 3, pj = pi & 7;
      if constexpr (j == 7 && ni < 128) {
        asm volatile("s_waitcnt vmcnt(%0)" ::"i"(kt + 2 < 16 ? 4 : 0) : "memory");
        tie2(kf[nkt % 3][0], kf[nkt % 3][1]); tie2(vf[nkt % 3][0], vf[nkt % 3][1]);
      }
      if constexpr (ni < 128) s0[ni & 1] = MFMA16(kf[nkt % 3][0], qf[nj], zero4);
      SB();
      ea[0] = __builtin_amdgcn_exp2f(s0[i & 1][0]); ea[1] = __builtin_amdgcn_exp2f(s0[i & 1][1]);
      SB();
      if constexpr (i > 0) p[pi & 1][3] = pack2p(eb[0], eb[1]);
      SB();
      if constexpr (ni < 128) s1[ni & 1] = MFMA16(kf[nkt % 3][1], qf[nj], zero4);
      SB();
      eb[0] = __builtin_amdgcn_exp2f(s0[i & 1][2]); eb[1] = __builtin_amdgcn_exp2f(s0[i & 1][3]);
      SB();
      p[i & 1][0] = pack2p(ea[0], ea[1]);
      if constexpr (j == 1 && kt + 2 < 16) loadkv(std::integral_constant<int, kt + 2>{});
      SB();
      if constexpr (i > 0) o[pj][0] = MFMA16(vf[pkt % 3][0], __builtin_bit_cast(bf16x8, p[pi & 1]), o[pj][0]);
      SB();
      ea[0] = __builtin_amdgcn_exp2f(s1[i & 1][0]); ea[1] = __builtin_amdgcn_exp2f(s1[i & 1][1]);
      SB();
      p[i & 1][1] = pack2p(eb[0], eb[1]);
      SB();
      if constexpr (i > 0) o[pj][1] = MFMA16(vf[pkt % 3][1], __builtin_bit_cast(bf16x8, p[pi & 1]), o[pj][1]);
      SB();
      eb[0] = __builtin_amdgcn_exp2f(s1[i & 1][2]); eb[1] = __builtin_amdgcn_exp2f(s1[i & 1][3]);
      SB();
      p[i & 1][2] = pack2p(ea[0], ea[1]);
      SB();
    });
    p[1][3] = pack2(eb[0], eb[1]);
    o[7][0] = MFMA16(vf[15 % 3][0], __builtin_bit_cast(bf16x8, p[1]), o[7][0]);
    o[7][1] = MFMA16(vf[15 % 3][1], __builtin_bit_cast(bf16x8, p[1]), o[7][1]);
  }

  TS(2);
  bool bad = false;
  {
    float am = 0.f;
#pragma unroll
    for (int j = 0; j < 8; ++j) {
      const float l = o[j][1][0];
      bad |= top && !(l >= 0x1p-80f && l <= 0x1p64f);
#pragma unroll
      for (int e = 0; e < 4; ++e) am += fabsf(o[j][0][e]) + fabsf(o[j][1][e]);
    }
    bad |= !(am <= 0x1p120f);
  }
  if (__any(bad)) {
#pragma unroll
    for (int j = 0; j < 8; ++j) {
      if (top) qf[j][4] = (__bf16)0.f;
      o[j][0] = zero4; o[j][1] = zero4;
      mref[j] = -INFINITY;
    }
#pragma unroll 1
    for (int kt = 0; kt < 16; ++kt) {
      const bf16x8 k0 = Kf[kt * 128], k1 = Kf[kt * 128 + 64], v0 = Vf[kt * 128], v1 = Vf[kt * 128 + 64];
#pragma unroll
      for (int j = 0; j < 8; ++j) {
        const f32x4 sa = MFMA16(k0, qf[j], zero4), sb = MFMA16(k1, qf[j], zero4);
        const float mn = fmaxf(mref[j], col_max(sa, sb));
        const float al = __builtin_amdgcn_exp2f(mref[j] - mn);
        mref[j] = mn;
        bf16x8 pp;
#pragma unroll
        for (int e = 0; e < 4; ++e) {
          pp[e] = (__bf16)__builtin_amdgcn_exp2f(sa[e] - mn);
          pp[4 + e] = (__bf16)__builtin_amdgcn_exp2f(sb[e] - mn);
          o[j][0][e] *= al; o[j][1][e] *= al;
        }
        o[j][0] = MFMA16(v0, pp, o[j][0]);
        o[j][1] = MFMA16(v1, pp, o[j][1]);
      }
    }
  }

  f32x4* part = (f32x4*)smem;
  float* mbuf = (float*)(smem + 65536);
#pragma unroll
  for (int j = 0; j < 8; ++j) {
    mbuf[(j * 4 + wave) * 64 + lane] = top ? mref[j] + __builtin_amdgcn_logf(o[j][1][0]) : mref[j];
    if ((j >> 1) != wave) {
      part[((j * 4 + wave) * 2 + 0) * 64 + lane] = o[j][0];
      part[((j * 4 + wave) * 2 + 1) * 64 + lane] = o[j][1];
    }
  }
  f32x4 mine[2][2];
#pragma unroll
  for (int jj = 0; jj < 2; ++jj)
#pragma unroll
    for (int dt = 0; dt < 2; ++dt)
      mine[jj][dt] = wave == 0 ? o[jj][dt] : (wave == 1 ? o[2 + jj][dt] : (wave == 2 ? o[4 + jj][dt] : o[6 + jj][dt]));
  __syncthreads();
  const int c = lane & 15, g = lane >> 4;
#pragma unroll
  for (int jj = 0; jj < 2; ++jj) {
    const int j = 2 * wave + jj;
    float mw[4];
#pragma unroll
    for (int w = 0; w < 4; ++w) mw[w] = mbuf[(j * 4 + w) * 64 + 48 + c];
    const float mm = fmaxf(fmaxf(mw[0], mw[1]), fmaxf(mw[2], mw[3]));
#pragma unroll
    for (int w = 0; w < 4; ++w) mw[w] = mbuf[(j * 4 + w) * 64 + c];
    f32x4 r[2] = {zero4, zero4};
#pragma unroll
    for (int w = 0; w < 4; ++w) {
      const float f = __builtin_amdgcn_exp2f(mw[w] - mm);
#pragma unroll
      for (int dt = 0; dt < 2; ++dt) {
        f32x4 t;
        if (w == wave) t = mine[jj][dt];
        else t = part[((j * 4 + w) * 2 + dt) * 64 + lane];
#pragma unroll
        for (int e = 0; e < 4; ++e) r[dt][e] += f * t[e];
      }
    }
    const float inv = 1.0f / __shfl(r[1][0], 48 + c);
    const unsigned x0 = pack2(r[0][0] * inv, r[0][1] * inv), x1 = pack2(r[0][2] * inv, r[0][3] * inv);
    unsigned y0 = pack2(r[1][0] * inv, r[1][1] * inv), y1 = pack2(r[1][2] * inv, r[1][3] * inv);
    if (top) { y0 = 0u; y1 = 0u; }
    auto sa = __builtin_amdgcn_permlane16_swap(x0, y0, false, false);
    auto sb = __builtin_amdgcn_permlane16_swap(x1, y1, false, false);
    char* op = wso + WS_O + ((size_t)(qb * 8 + j) * 16 + head) * 1024 + ((g & 1) * 2 + (g >> 1)) * 256 + c * 16;
    st16<WT2>(op, u32x4{(unsigned)sa[0], (unsigned)sb[0], (unsigned)sa[1], (unsigned)sb[1]});
  }
  TSEND(wso, 1);
}

__global__ void __launch_bounds__(256, 2)
k2_main(const __bf16* __restrict__ csr, const __bf16* __restrict__ csi, const __bf16* __restrict__ wo, char* __restrict__ ws) {
  extern __shared__ __attribute__((aligned(16))) char smem[];
  const int lane = threadIdx.x & 63;
  const int wave = __builtin_amdgcn_readfirstlane(threadIdx.x >> 6);
  const int bid = blockIdx.x;
  if (bid < 256) {
    attn_role(ws, ws, smem, bid, wave, lane);
  } else {
    TSDECL TS(0); TS(1);
    const int wg = bid - 256;
    const int xcd = wg & 7, idx = wg >> 3;
    const int ks = xcd >> 1, mt = (xcd & 1) * 8 + (idx >> 2), nq = idx & 3;
    const int m0 = mt * 128 + wave * 32;
    unsigned avoff[4];
    a_offsets<2>(avoff, m0, SEQ * 2, lane, SEQ - 1);
    const char* A = (const char*)(ks < 2 ? csr : csi) + (ks & 1) * 2048;
    const char* B = ws + WS_B + ((size_t)(ks * 32) * 4 + nq) * 7168;
    f32x4 acc[2][7];
#pragma unroll
    for (int m = 0; m < 2; ++m)
#pragma unroll
      for (int n = 0; n < 7; ++n) acc[m][n] = f32x4{0.f, 0.f, 0.f, 0.f};
    gemm_core<K2CFG, 2, 2, true>(A, avoff, B, 4 * 7168, smem, wave, lane, K2ROT, acc);
    TS(2);
    unsigned sink = 0;
    if (lane < 25) {
      const int L = (idx * 4 + wave) * 25 + lane;
      touch(sink, (const char*)wo + (L < 3136 ? L : 3135) * 128);
    }
    store_rows<2>(acc, smem + wave * 4096, 16384, ws + WS_P + ((size_t)ks * SEQ + m0) * (DM * 2) + nq * 224, DM * 2, lane);
    keep(sink);
    TSEND(ws, 1);
  }
}

__global__ void __launch_bounds__(256, 1)
k3_final(const __bf16* __restrict__ wo, const char* __restrict__ ws, __bf16* __restrict__ out) {
  extern __shared__ __attribute__((aligned(16))) char smem[];
  const int lane = threadIdx.x & 63;
  const int wave = __builtin_amdgcn_readfirstlane(threadIdx.x >> 6);
  const int bid = blockIdx.x;
  TSDECL TS(0); TS(1); TS(2);
  const int xcd = bid & 7, idx = bid >> 3;
  if (idx < 16) {
    const int rt = xcd * 4 + (idx >> 2), nq = idx & 3;
    const int m0 = rt * 64 + wave * 16;
    const unsigned avoff[2] = {(unsigned)(lane * 16), 0u};
    f32x4 acc[1][7];
#pragma unroll
    for (int n = 0; n < 7; ++n) acc[0][n] = f32x4{0.f, 0.f, 0.f, 0.f};
    gemm_core<K3CFG, 1, 3, true>(ws + WS_O + (size_t)(m0 >> 4) * 16384, avoff, (const char*)wo + nq * 224, DM * 2, smem, wave, lane,
                                    K3ROT, acc);
    store_rows<1>(acc, smem + 16 * 7168 + wave * 4096, 0, (char*)out + (size_t)m0 * (2 * DM * 2) + nq * 224, 2 * DM * 2, lane);
  } else {
    const int u0 = (xcd * 14 + idx - 16) * 1024 + threadIdx.x;
    u32x4 t[4][4];
#pragma unroll
    for (int n = 0; n < 4; ++n)
#pragma unroll
      for (int ks = 0; ks < 4; ++ks) t[n][ks] = *(const u32x4*)(ws + WS_P + (size_t)ks * (SEQ * DM * 2) + (size_t)(u0 + n * 256) * 16);
#pragma unroll
    for (int n = 0; n < 4; ++n) {
      u32x4 r;
#pragma unroll
      for (int e = 0; e < 4; ++e) {
        float lo = 0.f, hi = 0.f;
#pragma unroll
        for (int ks = 0; ks < 4; ++ks) {
          lo += __builtin_bit_cast(float, t[n][ks][e] << 16);
          hi += __builtin_bit_cast(float, t[n][ks][e] & 0xffff0000u);
        }
        r[e] = pack2(lo, hi);
      }
      const int u = u0 + n * 256, row = u / 56, ch = u - row * 56;
      st16<WT3>((char*)out + (size_t)row * (2 * DM * 2) + DM * 2 + ch * 16, r);
    }
  }
  TSEND(ws, 2);
}

extern "C" void kernel_launch(void* const* d_in, const int* in_sizes, int n_in, void* d_out, int out_size,
                              void* d_ws, size_t ws_size, hipStream_t stream) {
  const __bf16* xa = (const __bf16*)d_in[0];
  const __bf16* wqkv = (const __bf16*)d_in[1];
  const __bf16* wo = (const __bf16*)d_in[2];
  const __bf16* xb = (const __bf16*)d_in[3];
  const __bf16* csr = (const __bf16*)d_in[4];
  const __bf16* csi = (const __bf16*)d_in[5];
  const __bf16* whr = (const __bf16*)d_in[6];
  const __bf16* whi = (const __bf16*)d_in[7];
  char* ws = (char*)d_ws;
  constexpr int L1 = 3 * 24576 + 5 * 7168, L2 = 81920, L3 = 16 * 7168 + 4 * 4096;
  (void)hipFuncSetAttribute((const void*)k1_proj, hipFuncAttributeMaxDynamicSharedMemorySize, L1);
  (void)hipFuncSetAttribute((const void*)k2_main, hipFuncAttributeMaxDynamicSharedMemorySize, L2);
  (void)hipFuncSetAttribute((const void*)k3_final, hipFuncAttributeMaxDynamicSharedMemorySize, L3);
  k1_proj<<<240, 256, L1, stream>>>(xa, wqkv, xb, whr, whi, ws);
  k2_main<<<512, 256, L2, stream>>>(csr, csi, wo, ws);
  k3_final<<<240, 256, L3, stream>>>(wo, ws, (__bf16*)d_out);
}
```

```cpp
#include <hip/hip_runtime.h>
#include <hip/hip_bf16.h>
#include <stdint.h>
#include <utility>

typedef __bf16 bf16x8 __attribute__((ext_vector_type(8)));
typedef __bf16 bf16x4 __attribute__((ext_vector_type(4)));
typedef __bf16 bf16x2 __attribute__((ext_vector_type(2)));
typedef short s16x4 __attribute__((ext_vector_type(4)));
typedef float f32x4 __attribute__((ext_vector_type(4)));
typedef float f32x16 __attribute__((ext_vector_type(16)));
typedef unsigned u32x4 __attribute__((ext_vector_type(4)));
typedef unsigned u32x2 __attribute__((ext_vector_type(2)));
#define LDSP __attribute__((address_space(3)))

#define TSDECL
#define TS(i)
#define TSEND(wsb, kidx)

#define ATROT 0
#define GPRIO 0
#define K3ROT ((2 * nq + (idx >> 2)) & 7)
#define K3CFG 8, 4, 8
#define K2CFG 16, 3, 4
#define ABURST(mt) ((mt) == 2)
#define WT1 true
#define WT2 true
#define WT3 true
#define XSLOT 1
#define K2ROT 0
constexpr int SEQ = 2048, DM = 448, NH = 16, DH = 28;

constexpr size_t WS_Q = 0;
constexpr size_t WS_K = 2u << 20;
constexpr size_t WS_V = 4u << 20;
constexpr size_t WS_B = 6u << 20;
constexpr size_t WS_O = 10u << 20;
constexpr size_t WS_P = 12u << 20;

template <int... Is, class F>
__device__ __forceinline__ void static_for_impl(std::integer_sequence<int, Is...>, F&& f) {
  (f(std::integral_constant<int, Is>{}), ...);
}
template <int N, class F>
__device__ __forceinline__ void static_for(F&& f) {
  static_for_impl(std::make_integer_sequence<int, N>{}, static_cast<F&&>(f));
}

__device__ __forceinline__ unsigned pack2(float a, float b) {
  bf16x2 v; v[0] = (__bf16)a; v[1] = (__bf16)b;
  return __builtin_bit_cast(unsigned, v);
}

template <int NKB, int NSA, int NSB, int MT>
struct Sched {
  static constexpr int NS = 2 * NKB;
  static constexpr int VB = 2, VA = 2 * MT;
  static constexpr int PA = NSA >= NKB ? NKB : NSA - 1;
  static constexpr int PB = NSB >= NS ? NS : NSB - 1;
  static constexpr int PM = (PA > (PB + 1) / 2) ? PA : (PB + 1) / 2;
  static constexpr int wait(int s) {
    int pos = 0, endB = 0, endA = 0;
    for (int p = 0; p < PM; ++p) {
      if (p < PA) { pos += VA; if (p == s / 2) endA = pos; }
      if (2 * p < PB) { pos += VB; if (2 * p == s) endB = pos; }
      if (2 * p + 1 < PB) { pos += VB; if (2 * p + 1 == s) endB = pos; }
    }
    for (int it = 0; it < s; ++it) {
      if (it + PB < NS) { pos += VB; if (it + PB == s) endB = pos; }
      if (it / 2 + PA < NKB && !(ABURST(MT) && (it & 1))) { pos += ABURST(MT) ? 2 * MT : MT; if (it / 2 + PA == s / 2) endA = pos; }
    }
    int e = endB > endA ? endB : endA;
    return pos - e;
  }
};

template <bool NT, bool L48>
__device__ __forceinline__ void glds16(unsigned voff, const void* sbase, unsigned lds_dst) {
  unsigned keep;
#define GLDS_(pre, pol, post) \
  asm volatile("s_mov_b32 %0, m0\n\ts_mov_b32 m0, %3\n\t" pre "s_nop 0\n\tglobal_load_lds_dwordx4 %1, %2" pol "\n\t" post "s_mov_b32 m0, %0" \
               : "=&s"(keep) : "v"(voff), "s"(sbase), "s"(lds_dst) : "memory")
  if constexpr (L48) {
    if constexpr (NT) GLDS_("s_mov_b32 exec_hi, 0xffff\n\t", " nt", "s_mov_b32 exec_hi, -1\n\t");
    else GLDS_("s_mov_b32 exec_hi, 0xffff\n\t", "", "s_mov_b32 exec_hi, -1\n\t");
  } else {
    if constexpr (NT) GLDS_("", " nt", "");
    else GLDS_("", "", "");
  }
#undef GLDS_
}
#define NTA(bk) ((bk) == 2)
#define NTB(bk) false

template <bool WT>
__device__ __forceinline__ void st16(void* p, u32x4 v) {
  if constexpr (WT) asm volatile("global_store_dwordx4 %0, %1, off sc1\n\ts_nop 1" ::"v"(p), "v"(v) : "memory");
  else *(u32x4*)p = v;
}

__device__ __forceinline__ void touch(unsigned& sink, const void* p) {
  asm volatile("global_load_dword %0, %1, off" : "=v"(sink) : "v"(p) : "memory");
}
__device__ __forceinline__ const char* code_ptr() {
  unsigned long long pc;
  asm volatile("s_getpc_b64 %0" : "=s"(pc));
  return (const char*)(pc & ~127ull);
}
__device__ __forceinline__ void keep(unsigned& sink) { asm volatile("s_waitcnt vmcnt(0)" : "+v"(sink)::"memory"); }

template <int OFF>
__device__ __forceinline__ void gload16(bf16x8& dst, unsigned voff, const void* sbase) {
  asm volatile("global_load_dwordx4 %0, %1, %2 offset:%3" : "=v"(dst) : "v"(voff), "s"(sbase), "i"(OFF) : "memory");
}
__device__ __forceinline__ void tie2(bf16x8& x, bf16x8& y) { asm volatile("" : "+v"(x), "+v"(y)); }

template <int NKB, int NSA, int NSB, int MT, int BKIND, bool SWAP>
__device__ __forceinline__ void gemm_core(const char* Abase, const unsigned (&avoff)[2 * MT], const char* Bbase, int ldb2,
                                          char* smem, int wave, int lane, int rot, f32x4 (&acc)[MT][7]) {
  using SC = Sched<NKB, NSA, NSB, MT>;
  constexpr int NS = SC::NS, PA = SC::PA, PB = SC::PB;
  constexpr int ASLOT = MT * 8192;
  constexpr bool BTR = BKIND == 0 || BKIND == 3, ADIR = BKIND == 3;
  constexpr int BOFF = ADIR ? 0 : NSA * ASLOT;
  constexpr int NSLB = ADIR ? NS : (MT == 3 ? NSB + XSLOT : NSB);
  const unsigned lds0 = (unsigned)(uintptr_t)(LDSP char*)smem;
  unsigned bvoff[2];
  static_for<2>([&](auto P) {
    constexpr int p = P;
    int L = wave * 1792 + p * 1024 + lane * 16;
    if (p == 1 && lane >= 48) L = wave * 1792;
    if constexpr (BTR) {
      int pos = L / 224, ch = (L - pos * 224) >> 4;
      int k = (pos & 3) + 8 * ((pos >> 2) & 1) + 4 * ((pos >> 3) & 1) + (pos & 16);
      if constexpr (BKIND == 3) k = k < 28 ? k : 27;
      bvoff[p] = k * ldb2 + ch * 16;
    } else if constexpr (BKIND == 1) {
      int n = L >> 6, chp = (L >> 4) & 3;
      bvoff[p] = n * ldb2 + 16 * (chp ^ ((-(n >> 2)) & 3));
    } else {
      bvoff[p] = L;
    }
  });
  const int bstep = (BKIND == 0) ? 32 * ldb2 : (BKIND == 1 ? 64 : (BKIND == 2 ? ldb2 : 28 * ldb2));
  bf16x8 fd[ADIR ? NS : 1][MT];
  auto issueB1 = [&](auto S, auto P) {
    constexpr int s = S;
    int se = s + 2 * rot;
    se = se >= NS ? se - NS : se;
    const char* src = Bbase + (ptrdiff_t)se * bstep;
    const unsigned d = lds0 + BOFF + (s % NSLB) * 7168 + wave * 1792;
    if constexpr (P == 0) glds16<NTB(BKIND), false>(bvoff[0], src, d);
    else glds16<NTB(BKIND), true>(bvoff[1], src, d + 1024);
  };
  auto issueA1 = [&](auto KB, auto P) {
    constexpr int kb = KB;
    int ke = kb + rot;
    ke = ke >= NKB ? ke - NKB : ke;
    if constexpr (ADIR) {
      gload16<(P & 1) * 1024>(fd[2 * kb + (P & 1)][P >> 1], avoff[P & ~1], Abase + ke * 2048);
    } else {
      const char* src = Abase + ke * 128;
      const unsigned d = lds0 + (kb % NSA) * ASLOT + wave * (MT * 2048);
      glds16<NTA(BKIND), false>(avoff[P], src, d + P * 1024);
    }
  };
  auto issueB = [&](auto S) { static_for<2>([&](auto P) { issueB1(S, P); }); };
  auto issueA = [&](auto KB) { static_for<2 * MT>([&](auto P) { issueA1(KB, P); }); };
  auto stepload = [&](auto S, auto J) {
    constexpr int s = S, j = J;
    if constexpr (j < 2) {
      if constexpr (s + PB < NS) issueB1(std::integral_constant<int, s + PB>{}, J);
    } else {
      if constexpr (s / 2 + PA < NKB) {
        if constexpr (!ABURST(MT)) {
          issueA1(std::integral_constant<int, s / 2 + PA>{}, std::integral_constant<int, (s & 1) * MT + j - 2>{});
        } else if constexpr ((s & 1) == 0) {
          issueA1(std::integral_constant<int, s / 2 + PA>{}, std::integral_constant<int, 2 * (j - 2)>{});
          issueA1(std::integral_constant<int, s / 2 + PA>{}, std::integral_constant<int, 2 * (j - 2) + 1>{});
        }
      }
    }
  };
  static_for<SC::PM>([&](auto P) {
    constexpr int p = P;
    if constexpr (p < PA) issueA(P);
    if constexpr (2 * p < PB) issueB(std::integral_constant<int, 2 * p>{});
    if constexpr (2 * p + 1 < PB) issueB(std::integral_constant<int, 2 * p + 1>{});
  });
  const int c = lane & 15, g = lane >> 4;
  int rb;
  if constexpr (BTR) rb = ((c >> 2) + 4 * (g & 1) + 16 * (g >> 1)) * 224 + 8 * (c & 3);
  else if constexpr (BKIND == 1) rb = c * 64 + 16 * (g ^ ((-(c >> 2)) & 3));
  else rb = lane * 16;
  const int ra0 = wave * (MT * 2048) + c * 128 + 16 * (g ^ (c >> 1));
  const int ra1 = ra0 ^ 64;

  bf16x8 fa[2][MT], fb[2][7];
  auto sync = [&](auto S) {
    constexpr int s = S;
    if constexpr (NSLB > NSB) asm volatile("s_waitcnt vmcnt(%0)" ::"i"(SC::wait(s)) : "memory");
    else asm volatile("s_waitcnt vmcnt(%0) lgkmcnt(0)" ::"i"(SC::wait(s)) : "memory");
    __builtin_amdgcn_s_barrier();
    if constexpr (BKIND == 2 && s == 0 && GPRIO > 0) __builtin_amdgcn_s_setprio(1);
    if constexpr (BKIND == 2 && s == GPRIO && GPRIO > 0) __builtin_amdgcn_s_setprio(0);
    if constexpr (ADIR && (s & 1) == 0) static_for<MT>([&](auto M) { tie2(fd[s][M], fd[s + 1][M]); });
    __builtin_amdgcn_sched_barrier(0);
  };
  constexpr int NRA = ADIR ? 0 : MT;
  constexpr int NSIDE = NRA + 7 + 2 + MT;
  auto side = [&](auto S, auto K) {
    constexpr int s = S, k = K;
    if constexpr (k < NRA) {
      const char* sa = smem + ((s / 2) % NSA) * ASLOT + ((s & 1) ? ra1 : ra0);
      fa[s & 1][k] = *(const bf16x8*)(sa + k * 2048);
    } else if constexpr (k < NRA + 7) {
      constexpr int nt = k - NRA;
      const char* sb = smem + BOFF + (s % NSLB) * 7168 + rb;
      if constexpr (BTR) {
        s16x4 lo = __builtin_amdgcn_ds_read_tr16_b64_v4i16((LDSP s16x4*)(sb + 32 * nt));
        s16x4 hi = __builtin_amdgcn_ds_read_tr16_b64_v4i16((LDSP s16x4*)(sb + 8 * 224 + 32 * nt));
        typedef short s16x8 __attribute__((ext_vector_type(8)));
        s16x8 t = __builtin_shufflevector(lo, hi, 0, 1, 2, 3, 4, 5, 6, 7);
        fb[s & 1][nt] = __builtin_bit_cast(bf16x8, t);
      } else {
        fb[s & 1][nt] = *(const bf16x8*)(sb + nt * 1024);
      }
    } else {
      stepload(S, std::integral_constant<int, k - NRA - 7>{});
    }
    __builtin_amdgcn_sched_barrier(0);
  };
  sync(std::integral_constant<int, 0>{});
  static_for<NSIDE>([&](auto K) { side(std::integral_constant<int, 0>{}, K); });
  static_for<NS>([&](auto S) {
    constexpr int s = S;
    if constexpr (s + 1 < NS) sync(std::integral_constant<int, s + 1>{});
    static_for<7>([&](auto N) {
      constexpr int nt = N;
      static_for<MT>([&](auto M) {
        constexpr int m = M;
        const bf16x8 av = ADIR ? fd[ADIR ? s : 0][m] : fa[s & 1][m];
        if constexpr (SWAP)
          acc[m][nt] = __builtin_amdgcn_mfma_f32_16x16x32_bf16(fb[s & 1][nt], av, acc[m][nt], 0, 0, 0);
        else
          acc[m][nt] = __builtin_amdgcn_mfma_f32_16x16x32_bf16(av, fb[s & 1][nt], acc[m][nt], 0, 0, 0);
        __builtin_amdgcn_sched_barrier(0);
        if constexpr (s + 1 < NS) {
          static_for<NSIDE>([&](auto K) {
            if constexpr ((K * 7 * MT) / NSIDE == nt * MT + m) side(std::integral_constant<int, s + 1>{}, K);
          });
        }
      });
    });
  });
}

template <int MT>
__device__ __forceinline__ void a_offsets(unsigned (&avoff)[2 * MT], int m0, int lda2, int lane, int maxrow) {
#pragma unroll
  for (int p = 0; p < 2 * MT; ++p) {
    const int r = 8 * p + (lane >> 3);
    int row = m0 + r;
    row = row < maxrow ? row : maxrow;
    avoff[p] = row * lda2 + 16 * ((lane & 7) ^ ((r >> 1) & 7));
  }
}

template <int MT>
__device__ __forceinline__ void store_rows(const f32x4 (&acc)[MT][7], char* tb, int tstride, char* dst, int ld2, int lane) {
  const int c = lane & 15, g = lane >> 4;
#pragma unroll
  for (int m = 0; m < MT; ++m)
#pragma unroll
    for (int n = 0; n < 7; ++n)
      *(u32x2*)(tb + m * tstride + c * 240 + n * 32 + g * 8) = u32x2{pack2(acc[m][n][0], acc[m][n][1]), pack2(acc[m][n][2], acc[m][n][3])};
#pragma unroll
  for (int m = 0; m < MT; ++m)
#pragma unroll
    for (int i = 0; i < 4; ++i) {
      const int u = i * 64 + lane;
      const int row = (u * 4682) >> 16, ch = u - row * 14;
      if (i < 3 || lane < 32) {
        const u32x4 v = *(const u32x4*)(tb + m * tstride + row * 240 + ch * 16);
        st16<MT == 2 ? WT2 : WT3>(dst + (size_t)(m * 16 + row) * ld2 + ch * 16, v);
      }
    }
}

constexpr int K1_MT = 3;
#define K1CFG 7, 3, 4
__global__ void __launch_bounds__(256, 1)
k1_proj(const __bf16* __restrict__ xa, const __bf16* __restrict__ wqkv, const __bf16* __restrict__ xb,
        const __bf16* __restrict__ whr, const __bf16* __restrict__ whi, char* __restrict__ ws) {
  extern __shared__ __attribute__((aligned(16))) char smem[];
  const int lane = threadIdx.x & 63;
  const int wave = __builtin_amdgcn_readfirstlane(threadIdx.x >> 6);
  const int bid = blockIdx.x;
  TSDECL TS(0); TS(1);
  const int xcd = bid & 7, idx = bid >> 3;
  unsigned sink = 0;
  {
    const char* pc = code_ptr();
    const int L = (idx * 4 + wave) * 64 + lane;
    if (L < 320) touch(sink, pc + L * 128);
  }
  const int rt = idx / 10, ci = idx - 10 * rt;
  const int ct = (xcd & 1) * 10 + ci;
  const int lim = (xcd >> 1) * 512 + 512;
  const int m0 = (xcd >> 1) * 512 + rt * 192 + wave * 48;
  const int rot = (ci + 3 * rt) % 7;
  const int c = lane & 15, g = lane >> 4;
  unsigned avoff[2 * K1_MT];
  a_offsets<K1_MT>(avoff, m0, DM * 2, lane, lim - 1);
  f32x4 acc[K1_MT][7];
#pragma unroll
  for (int m = 0; m < K1_MT; ++m)
#pragma unroll
    for (int n = 0; n < 7; ++n) acc[m][n] = f32x4{0.f, 0.f, 0.f, 0.f};

  char* tb = smem + wave * 6144;
  if (ct < 8) {
    gemm_core<K1CFG, K1_MT, 0, true>((const char*)xa, avoff, (const char*)wqkv + ct * 224, 3 * DM * 2, smem, wave, lane, rot, acc);
    TS(2);
    const bool isq = ct < 4;
    const float sc = isq ? 0.18898223650461362f * 1.4426950408889634f : 1.0f;
    char* dst = ws + (isq ? WS_Q : WS_K);
    const unsigned padlo = isq ? 0u : 0x3f803f80u, padhi = isq ? 0u : 0x00003f80u;
#pragma unroll
    for (int m = 0; m < K1_MT; ++m) {
      char* st = tb + m * 24576;
#pragma unroll
      for (int nt = 0; nt < 7; ++nt) {
        const int n4 = 4 * nt + g;
        const int hh = (n4 * 37) >> 8;
        const int q4 = n4 - 7 * hh;
        char* p = st + (hh * 4 + (q4 >> 1)) * 256 + c * 16 + (q4 & 1) * 8;
        *(u32x2*)p = u32x2{pack2(acc[m][nt][0] * sc, acc[m][nt][1] * sc), pack2(acc[m][nt][2] * sc, acc[m][nt][3] * sc)};
        if (q4 == 6) *(u32x2*)(p + 8) = u32x2{padlo, padhi};
      }
    }
#pragma unroll
    for (int m = 0; m < K1_MT; ++m) {
      const int t0 = m0 + 16 * m;
      if (t0 < lim) {
#pragma unroll
        for (int hh = 0; hh < 4; ++hh) {
          const u32x4 v = *(const u32x4*)(tb + m * 24576 + hh * 1024 + lane * 16);
          const int lo = isq ? (t0 & 16) * 64 + c * 16 : ((c >> 2) & 1) * 1024 + ((t0 & 16) / 2 + (c >> 3) * 4 + (c & 3)) * 16;
          st16<WT1>(dst + ((4 * (ct & 3) + hh) * 64 + (t0 >> 5)) * 2048 + g * 256 + lo, v);
        }
      }
    }
  } else if (ct < 12) {
    gemm_core<K1CFG, K1_MT, 0, false>((const char*)xa, avoff, (const char*)wqkv + ct * 224, 3 * DM * 2, smem, wave, lane, rot, acc);
    TS(2);
    char* dst = ws + WS_V;
#pragma unroll
    for (int m = 0; m < K1_MT; ++m) {
      char* st = tb + m * 24576;
#pragma unroll
      for (int nt = 0; nt < 7; ++nt) {
        const int nl = 16 * nt + c;
        const int hh = (nl * 147) >> 12;
        const int d = nl - 28 * hh;
        char* p = st + hh * 1024 + (d >> 4) * 512 + (g >> 1) * 256 + (d & 15) * 16 + (g & 1) * 8;
        *(u32x2*)p = u32x2{pack2(acc[m][nt][0], acc[m][nt][1]), pack2(acc[m][nt][2], acc[m][nt][3])};
      }
      if (lane < 32) {
        const unsigned o1 = (lane & 3) == 0 ? 0x3f803f80u : 0u;
        *(u32x4*)(st + (lane >> 3) * 1024 + 512 + ((lane >> 2) & 1) * 256 + (12 + (lane & 3)) * 16) = u32x4{o1, o1, o1, o1};
      }
    }
#pragma unroll
    for (int m = 0; m < K1_MT; ++m) {
      const int t0 = m0 + 16 * m;
      if (t0 < lim) {
#pragma unroll
        for (int hh = 0; hh < 4; ++hh) {
          const u32x4 v = *(const u32x4*)(tb + m * 24576 + hh * 1024 + lane * 16);
          st16<WT1>(dst + ((4 * (ct & 3) + hh) * 64 + (t0 >> 5)) * 2048 + (lane >> 5) * 1024 + (t0 & 16) * 32 + (lane & 31) * 16, v);
        }
      }
    }
  } else {
    const bool isb = ct >= 16;
    const int nq = ct & 3;
    const char* W = (const char*)(isb ? whi : whr) + (size_t)nq * 112 * (DM * 2);
    gemm_core<K1CFG, K1_MT, 1, false>((const char*)xb, avoff, W, DM * 2, smem, wave, lane, rot, acc);
    TS(2);
    const float sg = isb ? -1.f : 1.f;
    char* dst = ws + WS_B;
#pragma unroll
    for (int m = 0; m < K1_MT; ++m) {
      char* st = tb + m * 24576;
#pragma unroll
      for (int nt = 0; nt < 7; ++nt)
        *(u32x2*)(st + nt * 512 + (g >> 1) * 256 + c * 16 + (g & 1) * 8) =
            u32x2{pack2(acc[m][nt][0] * sg, acc[m][nt][1] * sg), pack2(acc[m][nt][2] * sg, acc[m][nt][3] * sg)};
    }
#pragma unroll
    for (int m = 0; m < K1_MT; ++m) {
      const int t0 = m0 + 16 * m;
      if (t0 < lim) {
        const int kk = t0 + (isb ? SEQ : 0);
        char* gp = dst + ((kk >> 5) * 4 + nq) * 7168 + (kk & 16) * 32 + (lane >> 5) * 1024 + (lane & 31) * 16;
#pragma unroll
        for (int i = 0; i < 4; ++i) {
          if (i < 3 || lane < 32) {
            const u32x4 v = *(const u32x4*)(tb + m * 24576 + i * 1024 + lane * 16);
            st16<WT1>(gp + i * 2048, v);
          }
        }
      }
    }
  }
  keep(sink);
  TSEND(ws, 0);
}

__device__ __forceinline__ float col_max(const f32x4& a, const f32x4& b) {
  float m = fmaxf(fmaxf(fmaxf(a[0], a[1]), fmaxf(a[2], a[3])), fmaxf(fmaxf(b[0], b[1]), fmaxf(b[2], b[3])));
  auto r = __builtin_amdgcn_permlane32_swap(__builtin_bit_cast(unsigned, m), __builtin_bit_cast(unsigned, m), false, false);
  m = fmaxf(__builtin_bit_cast(float, (unsigned)r[0]), __builtin_bit_cast(float, (unsigned)r[1]));
  auto t = __builtin_amdgcn_permlane16_swap(__builtin_bit_cast(unsigned, m), __builtin_bit_cast(unsigned, m), false, false);
  return fmaxf(__builtin_bit_cast(float, (unsigned)t[0]), __builtin_bit_cast(float, (unsigned)t[1]));
}
__device__ __forceinline__ unsigned pack2p(float a, float b) {
  unsigned r = pack2(a, b);
  asm volatile("" : "+v"(r));
  return r;
}
#define MFMA16(a, b, c) __builtin_amdgcn_mfma_f32_16x16x32_bf16(a, b, c, 0, 0, 0)

__device__ __forceinline__ void attn_role(const char* __restrict__ ws, char* __restrict__ wso, char* smem, int wg, int wave, int lane) {
  TSDECL TS(0);
  const int xcd = wg & 7, idx = wg >> 3;
  const int head = xcd * 2 + (idx >> 4), qb = idx & 15;
  const bf16x8* Kf = (const bf16x8*)(ws + WS_K) + (size_t)(head * 64 + wave * 16) * 128 + lane;
  const bf16x8* Vf = (const bf16x8*)(ws + WS_V) + (size_t)(head * 64 + wave * 16) * 128 + lane;
  const f32x4 zero4 = {0.f, 0.f, 0.f, 0.f};

  const char* Kb = (const char*)(ws + WS_K) + (size_t)(head * 64 + wave * 16) * 2048;
  const unsigned l16 = lane * 16;
  bf16x8 kf[3][2], vf[3][2];
  bf16x8 qf[8];
  auto loadkv = [&](auto T) {
    constexpr int t = T;
    const char* src = Kb + t * 2048;
    gload16<0>(kf[t % 3][0], l16, src);
    gload16<1024>(kf[t % 3][1], l16, src);
    gload16<0>(vf[t % 3][0], l16, src + (WS_V - WS_K));
    gload16<1024>(vf[t % 3][1], l16, src + (WS_V - WS_K));
  };
  loadkv(std::integral_constant<int, 0>{});
  {
    const char* Qb = (const char*)(ws + WS_Q) + (size_t)(head * 64 + qb * 4) * 2048;
    static_for<4>([&](auto QT) {
      gload16<0>(qf[2 * QT], l16, Qb + QT * 2048);
      gload16<1024>(qf[2 * QT + 1], l16, Qb + QT * 2048);
    });
  }
  loadkv(std::integral_constant<int, 1>{});
  asm volatile("s_waitcnt vmcnt(4)" ::: "memory");
  tie2(kf[0][0], kf[0][1]); tie2(vf[0][0], vf[0][1]);
#pragma unroll
  for (int j = 0; j < 4; ++j) tie2(qf[2 * j], qf[2 * j + 1]);
  const bool top = lane >= 48;
  float mref[8];
#pragma unroll
  for (int j = 0; j < 8; ++j) {
    const f32x4 sa = MFMA16(kf[0][0], qf[j], zero4), sb = MFMA16(kf[0][1], qf[j], zero4);
    const float nm = -(col_max(sa, sb) + 64.f);
    const __bf16 b0 = (__bf16)nm;
    mref[j] = -(float)b0;
    if (top) qf[j][4] = b0;
  }
  TS(1);
  f32x4 o[8][2];
#pragma unroll
  for (int j = 0; j < 8; ++j) { o[j][0] = zero4; o[j][1] = zero4; }

  {
#define SB() __builtin_amdgcn_sched_barrier(0)
    f32x4 s0[2], s1[2];
    u32x4 p[2];
    float ea[2], eb[2];
    s0[0] = MFMA16(kf[0][0], qf[0], zero4);
    s1[0] = MFMA16(kf[0][1], qf[0], zero4);
    SB();
    static_for<128>([&](auto I) {
      constexpr int i = I, kt = i >> 3, j = i & 7;
      constexpr int ni = i + 1, nkt = ni >> 3, nj = ni & 7;
      constexpr int pi = i - 1, pkt = pi >> 3, pj = pi & 7;
      if constexpr (j == 7 && ni < 128) {
        asm volatile("s_waitcnt vmcnt(%0)" ::"i"(kt + 2 < 16 ? 4 : 0) : "memory");
        tie2(kf[nkt % 3][0], kf[nkt % 3][1]); tie2(vf[nkt % 3][0], vf[nkt % 3][1]);
      }
      if constexpr (ni < 128) s0[ni & 1] = MFMA16(kf[nkt % 3][0], qf[nj], zero4);
      SB();
      ea[0] = __builtin_amdgcn_exp2f(s0[i & 1][0]); ea[1] = __builtin_amdgcn_exp2f(s0[i & 1][1]);
      SB();
      if constexpr (i > 0) p[pi & 1][3] = pack2p(eb[0], eb[1]);
      SB();
      if constexpr (ni < 128) s1[ni & 1] = MFMA16(kf[nkt % 3][1], qf[nj], zero4);
      SB();
      eb[0] = __builtin_amdgcn_exp2f(s0[i & 1][2]); eb[1] = __builtin_amdgcn_exp2f(s0[i & 1][3]);
      SB();
      p[i & 1][0] = pack2p(ea[0], ea[1]);
      if constexpr (j == 1 && kt + 2 < 16) loadkv(std::integral_constant<int, kt + 2>{});
      SB();
      if constexpr (i > 0) o[pj][0] = MFMA16(vf[pkt % 3][0], __builtin_bit_cast(bf16x8, p[pi & 1]), o[pj][0]);
      SB();
      ea[0] = __builtin_amdgcn_exp2f(s1[i & 1][0]); ea[1] = __builtin_amdgcn_exp2f(s1[i & 1][1]);
      SB();
      p[i & 1][1] = pack2p(eb[0], eb[1]);
      SB();
      if constexpr (i > 0) o[pj][1] = MFMA16(vf[pkt % 3][1], __builtin_bit_cast(bf16x8, p[pi & 1]), o[pj][1]);
      SB();
      eb[0] = __builtin_amdgcn_exp2f(s1[i & 1][2]); eb[1] = __builtin_amdgcn_exp2f(s1[i & 1][3]);
      SB();
      p[i & 1][2] = pack2p(ea[0], ea[1]);
      SB();
    });
    p[1][3] = pack2(eb[0], eb[1]);
    o[7][0] = MFMA16(vf[15 % 3][0], __builtin_bit_cast(bf16x8, p[1]), o[7][0]);
    o[7][1] = MFMA16(vf[15 % 3][1], __builtin_bit_cast(bf16x8, p[1]), o[7][1]);
  }

  TS(2);
  bool bad = false;
  {
    float am = 0.f;
#pragma unroll
    for (int j = 0; j < 8; ++j) {
      const float l = o[j][1][0];
      bad |= top && !(l >= 0x1p-80f && l <= 0x1p64f);
#pragma unroll
      for (int e = 0; e < 4; ++e) am += fabsf(o[j][0][e]) + fabsf(o[j][1][e]);
    }
    bad |= !(am <= 0x1p120f);
  }
  if (__any(bad)) {
#pragma unroll
    for (int j = 0; j < 8; ++j) {
      if (top) qf[j][4] = (__bf16)0.f;
      o[j][0] = zero4; o[j][1] = zero4;
      mref[j] = -INFINITY;
    }
#pragma unroll 1
    for (int kt = 0; kt < 16; ++kt) {
      const bf16x8 k0 = Kf[kt * 128], k1 = Kf[kt * 128 + 64], v0 = Vf[kt * 128], v1 = Vf[kt * 128 + 64];
#pragma unroll
      for (int j = 0; j < 8; ++j) {
        const f32x4 sa = MFMA16(k0, qf[j], zero4), sb = MFMA16(k1, qf[j], zero4);
        const float mn = fmaxf(mref[j], col_max(sa, sb));
        const float al = __builtin_amdgcn_exp2f(mref[j] - mn);
        mref[j] = mn;
        bf16x8 pp;
#pragma unroll
        for (int e = 0; e < 4; ++e) {
          pp[e] = (__bf16)__builtin_amdgcn_exp2f(sa[e] - mn);
          pp[4 + e] = (__bf16)__builtin_amdgcn_exp2f(sb[e] - mn);
          o[j][0][e] *= al; o[j][1][e] *= al;
        }
        o[j][0] = MFMA16(v0, pp, o[j][0]);
        o[j][1] = MFMA16(v1, pp, o[j][1]);
      }
    }
  }

  f32x4* part = (f32x4*)smem;
  float* mbuf = (float*)(smem + 65536);
#pragma unroll
  for (int j = 0; j < 8; ++j) {
    mbuf[(j * 4 + wave) * 64 + lane] = top ? mref[j] + __builtin_amdgcn_logf(o[j][1][0]) : mref[j];
    if ((j >> 1) != wave) {
      part[((j * 4 + wave) * 2 + 0) * 64 + lane] = o[j][0];
      part[((j * 4 + wave) * 2 + 1) * 64 + lane] = o[j][1];
    }
  }
  f32x4 mine[2][2];
#pragma unroll
  for (int jj = 0; jj < 2; ++jj)
#pragma unroll
    for (int dt = 0; dt < 2; ++dt)
      mine[jj][dt] = wave == 0 ? o[jj][dt] : (wave == 1 ? o[2 + jj][dt] : (wave == 2 ? o[4 + jj][dt] : o[6 + jj][dt]));
  __syncthreads();
  const int c = lane & 15, g = lane >> 4;
#pragma unroll
  for (int jj = 0; jj < 2; ++jj) {
    const int j = 2 * wave + jj;
    float mw[4];
#pragma unroll
    for (int w = 0; w < 4; ++w) mw[w] = mbuf[(j * 4 + w) * 64 + 48 + c];
    const float mm = fmaxf(fmaxf(mw[0], mw[1]), fmaxf(mw[2], mw[3]));
#pragma unroll
    for (int w = 0; w < 4; ++w) mw[w] = mbuf[(j * 4 + w) * 64 + c];
    f32x4 r[2] = {zero4, zero4};
#pragma unroll
    for (int w = 0; w < 4; ++w) {
      const float f = __builtin_amdgcn_exp2f(mw[w] - mm);
#pragma unroll
      for (int dt = 0; dt < 2; ++dt) {
        f32x4 t;
        if (w == wave) t = mine[jj][dt];
        else t = part[((j * 4 + w) * 2 + dt) * 64 + lane];
#pragma unroll
        for (int e = 0; e < 4; ++e) r[dt][e] += f * t[e];
      }
    }
    const float inv = 1.0f / __shfl(r[1][0], 48 + c);
    const unsigned x0 = pack2(r[0][0] * inv, r[0][1] * inv), x1 = pack2(r[0][2] * inv, r[0][3] * inv);
    unsigned y0 = pack2(r[1][0] * inv, r[1][1] * inv), y1 = pack2(r[1][2] * inv, r[1][3] * inv);
    if (top) { y0 = 0u; y1 = 0u; }
    auto sa = __builtin_amdgcn_permlane16_swap(x0, y0, false, false);
    auto sb = __builtin_amdgcn_permlane16_swap(x1, y1, false, false);
    char* op = wso + WS_O + ((size_t)(qb * 8 + j) * 16 + head) * 1024 + ((g & 1) * 2 + (g >> 1)) * 256 + c * 16;
    st16<WT2>(op, u32x4{(unsigned)sa[0], (unsigned)sb[0], (unsigned)sa[1], (unsigned)sb[1]});
  }
  TSEND(wso, 1);
}

__global__ void __launch_bounds__(256, 2)
k2_main(const __bf16* __restrict__ csr, const __bf16* __restrict__ csi, const __bf16* __restrict__ wo, char* __restrict__ ws,
        __bf16* __restrict__ out) {
  extern __shared__ __attribute__((aligned(16))) char smem[];
  const int lane = threadIdx.x & 63;
  const int wave = __builtin_amdgcn_readfirstlane(threadIdx.x >> 6);
  const int bid = blockIdx.x;
  const char* pc = code_ptr();
  if (bid < 256) {
    attn_role(ws, ws, smem, bid, wave, lane);
  } else {
    TSDECL TS(0); TS(1);
    const int wg = bid - 256;
    const int xcd = wg & 7, idx = wg >> 3;
    const int ks = xcd >> 1, mt = (xcd & 1) * 8 + (idx >> 2), nq = idx & 3;
    const int m0 = mt * 128 + wave * 32;
    unsigned sink0 = 0;
    {
      const int L = (idx * 4 + wave) * 64 + lane;
      if (L < 320) touch(sink0, pc + L * 128);
    }
    unsigned avoff[4];
    a_offsets<2>(avoff, m0, SEQ * 2, lane, SEQ - 1);
    const char* A = (const char*)(ks < 2 ? csr : csi) + (ks & 1) * 2048;
    const char* B = ws + WS_B + ((size_t)(ks * 32) * 4 + nq) * 7168;
    f32x4 acc[2][7];
#pragma unroll
    for (int m = 0; m < 2; ++m)
#pragma unroll
      for (int n = 0; n < 7; ++n) acc[m][n] = f32x4{0.f, 0.f, 0.f, 0.f};
    gemm_core<K2CFG, 2, 2, true>(A, avoff, B, 4 * 7168, smem, wave, lane, K2ROT, acc);
    TS(2);
    unsigned sink = 0;
    if (lane < 25) {
      const int L = (idx * 4 + wave) * 25 + lane;
      touch(sink, (const char*)wo + (L < 3136 ? L : 3135) * 128);
    }
    char* pd = ks == 0 ? (char*)out + (size_t)m0 * (4 * DM) + DM * 2 : ws + WS_P + ((size_t)ks * SEQ + m0) * (DM * 2);
    store_rows<2>(acc, smem + wave * 4096, 16384, pd + nq * 224, ks == 0 ? 4 * DM : 2 * DM, lane);
    keep(sink); keep(sink0);
    TSEND(ws, 1);
  }
}

__global__ void __launch_bounds__(256, 1)
k3_final(const __bf16* __restrict__ wo, const char* __restrict__ ws, __bf16* __restrict__ out) {
  extern __shared__ __attribute__((aligned(16))) char smem[];
  const int lane = threadIdx.x & 63;
  const int wave = __builtin_amdgcn_readfirstlane(threadIdx.x >> 6);
  const int bid = blockIdx.x;
  TSDECL TS(0); TS(1); TS(2);
  const int xcd = bid & 7, idx = bid >> 3;
  const char* pc = code_ptr();
  if (idx < 16) {
    const int rt = xcd * 4 + (idx >> 2), nq = idx & 3;
    const int m0 = rt * 64 + wave * 16;
    const unsigned avoff[2] = {(unsigned)(lane * 16), 0u};
    f32x4 acc[1][7];
#pragma unroll
    for (int n = 0; n < 7; ++n) acc[0][n] = f32x4{0.f, 0.f, 0.f, 0.f};
    gemm_core<K3CFG, 1, 3, true>(ws + WS_O + (size_t)(m0 >> 4) * 16384, avoff, (const char*)wo + nq * 224, DM * 2, smem, wave, lane,
                                    K3ROT, acc);
    store_rows<1>(acc, smem + 16 * 7168 + wave * 4096, 0, (char*)out + (size_t)m0 * (2 * DM * 2) + nq * 224, 2 * DM * 2, lane);
  } else {
    const int u0 = (xcd * 14 + idx - 16) * 1024 + threadIdx.x;
    u32x4 t[4][4];
    unsigned sink = 0;
    if (idx == 16 && threadIdx.x < 80) touch(sink, pc + threadIdx.x * 128);
    char* op[4];
#pragma unroll
    for (int n = 0; n < 4; ++n) {
      const int u = u0 + n * 256, row = u / 56, ch = u - row * 56;
      op[n] = (char*)out + (size_t)row * (2 * DM * 2) + DM * 2 + ch * 16;
      t[n][0] = *(const u32x4*)op[n];
#pragma unroll
      for (int ks = 1; ks < 4; ++ks) t[n][ks] = *(const u32x4*)(ws + WS_P + (size_t)ks * (SEQ * DM * 2) + (size_t)u * 16);
    }
#pragma unroll
    for (int n = 0; n < 4; ++n) {
      u32x4 r;
#pragma unroll
      for (int e = 0; e < 4; ++e) {
        float lo = 0.f, hi = 0.f;
#pragma unroll
        for (int ks = 0; ks < 4; ++ks) {
          lo += __builtin_bit_cast(float, t[n][ks][e] << 16);
          hi += __builtin_bit_cast(float, t[n][ks][e] & 0xffff0000u);
        }
        r[e] = pack2(lo, hi);
      }
      st16<WT3>(op[n], r);
    }
    keep(sink);
  }
  TSEND(ws, 2);
}

extern "C" void kernel_launch(void* const* d_in, const int* in_sizes, int n_in, void* d_out, int out_size,
                              void* d_ws, size_t ws_size, hipStream_t stream) {
  const __bf16* xa = (const __bf16*)d_in[0];
  const __bf16* wqkv = (const __bf16*)d_in[1];
  const __bf16* wo = (const __bf16*)d_in[2];
  const __bf16* xb = (const __bf16*)d_in[3];
  const __bf16* csr = (const __bf16*)d_in[4];
  const __bf16* csi = (const __bf16*)d_in[5];
  const __bf16* whr = (const __bf16*)d_in[6];
  const __bf16* whi = (const __bf16*)d_in[7];
  char* ws = (char*)d_ws;
  constexpr int L1 = 3 * 24576 + 5 * 7168, L2 = 81920, L3 = 16 * 7168 + 4 * 4096;
  (void)hipFuncSetAttribute((const void*)k1_proj, hipFuncAttributeMaxDynamicSharedMemorySize, L1);
  (void)hipFuncSetAttribute((const void*)k2_main, hipFuncAttributeMaxDynamicSharedMemorySize, L2);
  (void)hipFuncSetAttribute((const void*)k3_final, hipFuncAttributeMaxDynamicSharedMemorySize, L3);
  k1_proj<<<240, 256, L1, stream>>>(xa, wqkv, xb, whr, whi, ws);
  k2_main<<<512, 256, L2, stream>>>(csr, csi, wo, ws, (__bf16*)d_out);
  k3_final<<<240, 256, L3, stream>>>(wo, ws, (__bf16*)d_out);
}
```

```cpp
#include <hip/hip_runtime.h>
#include <hip/hip_bf16.h>
#include <stdint.h>
#include <utility>

typedef __bf16 bf16x8 __attribute__((ext_vector_type(8)));
typedef __bf16 bf16x4 __attribute__((ext_vector_type(4)));
typedef __bf16 bf16x2 __attribute__((ext_vector_type(2)));
typedef short s16x4 __attribute__((ext_vector_type(4)));
typedef float f32x4 __attribute__((ext_vector_type(4)));
typedef float f32x16 __attribute__((ext_vector_type(16)));
typedef unsigned u32x4 __attribute__((ext_vector_type(4)));
typedef unsigned u32x2 __attribute__((ext_vector_type(2)));
#define LDSP __attribute__((address_space(3)))

#define TSDECL
#define TS(i)
#define TSEND(wsb, kidx)

#define ATROT 0
#define GPRIO 0
#define K3ROT ((2 * nq + (idx >> 2)) & 7)
#define K3CFG 8, 4, 8
#define K2CFG 16, 3, 4
#define ABURST(mt) ((mt) == 2)
#define WT1 true
#define WT2 true
#define WT3 true
#define XSLOT 1
#define K2ROT 0
constexpr int SEQ = 2048, DM = 448, NH = 16, DH = 28;

constexpr size_t WS_Q = 0;
constexpr size_t WS_K = 2u << 20;
constexpr size_t WS_V = 4u << 20;
constexpr size_t WS_B = 6u << 20;
constexpr size_t WS_O = 10u << 20;
constexpr size_t WS_P = 12u << 20;

template <int... Is, class F>
__device__ __forceinline__ void static_for_impl(std::integer_sequence<int, Is...>, F&& f) {
  (f(std::integral_constant<int, Is>{}), ...);
}
template <int N, class F>
__device__ __forceinline__ void static_for(F&& f) {
  static_for_impl(std::make_integer_sequence<int, N>{}, static_cast<F&&>(f));
}

__device__ __forceinline__ unsigned pack2(float a, float b) {
  bf16x2 v; v[0] = (__bf16)a; v[1] = (__bf16)b;
  return __builtin_bit_cast(unsigned, v);
}

template <int NKB, int NSA, int NSB, int MT>
struct Sched {
  static constexpr int NS = 2 * NKB;
  static constexpr int VB = 2, VA = 2 * MT;
  static constexpr int PA = NSA >= NKB ? NKB : NSA - 1;
  static constexpr int PB = NSB >= NS ? NS : NSB - 1;
  static constexpr int PM = (PA > (PB + 1) / 2) ? PA : (PB + 1) / 2;
  static constexpr int wait(int s) {
    int pos = 0, endB = 0, endA = 0;
    for (int p = 0; p < PM; ++p) {
      if (p < PA) { pos += VA; if (p == s / 2) endA = pos; }
      if (2 * p < PB) { pos += VB; if (2 * p == s) endB = pos; }
      if (2 * p + 1 < PB) { pos += VB; if (2 * p + 1 == s) endB = pos; }
    }
    for (int it = 0; it < s; ++it) {
      if (it + PB < NS) { pos += VB; if (it + PB == s) endB = pos; }
      if (it / 2 + PA < NKB && !(ABURST(MT) && (it & 1))) { pos += ABURST(MT) ? 2 * MT : MT; if (it / 2 + PA == s / 2) endA = pos; }
    }
    int e = endB > endA ? endB : endA;
    return pos - e;
  }
};

template <bool NT, bool L48>
__device__ __forceinline__ void glds16(unsigned voff, const void* sbase, unsigned lds_dst) {
  unsigned keep;
#define GLDS_(pre, pol, post) \
  asm volatile("s_mov_b32 %0, m0\n\ts_mov_b32 m0, %3\n\t" pre "s_nop 0\n\tglobal_load_lds_dwordx4 %1, %2" pol "\n\t" post "s_mov_b32 m0, %0" \
               : "=&s"(keep) : "v"(voff), "s"(sbase), "s"(lds_dst) : "memory")
  if constexpr (L48) {
    if constexpr (NT) GLDS_("s_mov_b32 exec_hi, 0xffff\n\t", " nt", "s_mov_b32 exec_hi, -1\n\t");
    else GLDS_("s_mov_b32 exec_hi, 0xffff\n\t", "", "s_mov_b32 exec_hi, -1\n\t");
  } else {
    if constexpr (NT) GLDS_("", " nt", "");
    else GLDS_("", "", "");
  }
#undef GLDS_
}
#define NTA(bk) ((bk) == 2)
#define NTB(bk) false

template <bool WT>
__device__ __forceinline__ void st16(void* p, u32x4 v) {
  if constexpr (WT) asm volatile("global_store_dwordx4 %0, %1, off sc1\n\ts_nop 1" ::"v"(p), "v"(v) : "memory");
  else *(u32x4*)p = v;
}

__device__ __forceinline__ void touch(unsigned& sink, const void* p) {
  asm volatile("global_load_dword %0, %1, off" : "=v"(sink) : "v"(p) : "memory");
}
#define IC1 580
#define IC2 338
#define IC3 66
__device__ __forceinline__ const char* code_ptr() {
  unsigned long long pc;
  asm volatile("s_getpc_b64 %0" : "=s"(pc));
  return (const char*)(pc & ~127ull);
}
__device__ __forceinline__ void keep(unsigned& sink) { asm volatile("s_waitcnt vmcnt(0)" : "+v"(sink)::"memory"); }

template <int OFF>
__device__ __forceinline__ void gload16(bf16x8& dst, unsigned voff, const void* sbase) {
  asm volatile("global_load_dwordx4 %0, %1, %2 offset:%3" : "=v"(dst) : "v"(voff), "s"(sbase), "i"(OFF) : "memory");
}
__device__ __forceinline__ void tie2(bf16x8& x, bf16x8& y) { asm volatile("" : "+v"(x), "+v"(y)); }

template <int NKB, int NSA, int NSB, int MT, int BKIND, bool SWAP>
__device__ __forceinline__ void gemm_core(const char* Abase, const unsigned (&avoff)[2 * MT], const char* Bbase, int ldb2,
                                          char* smem, int wave, int lane, int rot, f32x4 (&acc)[MT][7]) {
  using SC = Sched<NKB, NSA, NSB, MT>;
  constexpr int NS = SC::NS, PA = SC::PA, PB = SC::PB;
  constexpr int ASLOT = MT * 8192;
  constexpr bool BTR = BKIND == 0 || BKIND == 3, ADIR = BKIND == 3;
  constexpr int BOFF = ADIR ? 0 : NSA * ASLOT;
  constexpr int NSLB = ADIR ? NS : (MT == 3 ? NSB + XSLOT : NSB);
  const unsigned lds0 = (unsigned)(uintptr_t)(LDSP char*)smem;
  unsigned bvoff[2];
  static_for<2>([&](auto P) {
    constexpr int p = P;
    int L = wave * 1792 + p * 1024 + lane * 16;
    if (p == 1 && lane >= 48) L = wave * 1792;
    if constexpr (BTR) {
      int pos = L / 224, ch = (L - pos * 224) >> 4;
      int k = (pos & 3) + 8 * ((pos >> 2) & 1) + 4 * ((pos >> 3) & 1) + (pos & 16);
      if constexpr (BKIND == 3) k = k < 28 ? k : 27;
      bvoff[p] = k * ldb2 + ch * 16;
    } else if constexpr (BKIND == 1) {
      int n = L >> 6, chp = (L >> 4) & 3;
      bvoff[p] = n * ldb2 + 16 * (chp ^ ((-(n >> 2)) & 3));
    } else {
      bvoff[p] = L;
    }
  });
  const int bstep = (BKIND == 0) ? 32 * ldb2 : (BKIND == 1 ? 64 : (BKIND == 2 ? ldb2 : 28 * ldb2));
  bf16x8 fd[ADIR ? NS : 1][MT];
  auto issueB1 = [&](auto S, auto P) {
    constexpr int s = S;
    int se = s + 2 * rot;
    se = se >= NS ? se - NS : se;
    const char* src = Bbase + (ptrdiff_t)se * bstep;
    const unsigned d = lds0 + BOFF + (s % NSLB) * 7168 + wave * 1792;
    if constexpr (P == 0) glds16<NTB(BKIND), false>(bvoff[0], src, d);
    else glds16<NTB(BKIND), true>(bvoff[1], src, d + 1024);
  };
  auto issueA1 = [&](auto KB, auto P) {
    constexpr int kb = KB;
    int ke = kb + rot;
    ke = ke >= NKB ? ke - NKB : ke;
    if constexpr (ADIR) {
      gload16<(P & 1) * 1024>(fd[2 * kb + (P & 1)][P >> 1], avoff[P & ~1], Abase + ke * 2048);
    } else {
      const char* src = Abase + ke * 128;
      const unsigned d = lds0 + (kb % NSA) * ASLOT + wave * (MT * 2048);
      glds16<NTA(BKIND), false>(avoff[P], src, d + P * 1024);
    }
  };
  auto issueB = [&](auto S) { static_for<2>([&](auto P) { issueB1(S, P); }); };
  auto issueA = [&](auto KB) { static_for<2 * MT>([&](auto P) { issueA1(KB, P); }); };
  auto stepload = [&](auto S, auto J) {
    constexpr int s = S, j = J;
    if constexpr (j < 2) {
      if constexpr (s + PB < NS) issueB1(std::integral_constant<int, s + PB>{}, J);
    } else {
      if constexpr (s / 2 + PA < NKB) {
        if constexpr (!ABURST(MT)) {
          issueA1(std::integral_constant<int, s / 2 + PA>{}, std::integral_constant<int, (s & 1) * MT + j - 2>{});
        } else if constexpr ((s & 1) == 0) {
          issueA1(std::integral_constant<int, s / 2 + PA>{}, std::integral_constant<int, 2 * (j - 2)>{});
          issueA1(std::integral_constant<int, s / 2 + PA>{}, std::integral_constant<int, 2 * (j - 2) + 1>{});
        }
      }
    }
  };
  static_for<SC::PM>([&](auto P) {
    constexpr int p = P;
    if constexpr (p < PA) issueA(P);
    if constexpr (2 * p < PB) issueB(std::integral_constant<int, 2 * p>{});
    if constexpr (2 * p + 1 < PB) issueB(std::integral_constant<int, 2 * p + 1>{});
  });
  const int c = lane & 15, g = lane >> 4;
  int rb;
  if constexpr (BTR) rb = ((c >> 2) + 4 * (g & 1) + 16 * (g >> 1)) * 224 + 8 * (c & 3);
  else if constexpr (BKIND == 1) rb = c * 64 + 16 * (g ^ ((-(c >> 2)) & 3));
  else rb = lane * 16;
  const int ra0 = wave * (MT * 2048) + c * 128 + 16 * (g ^ (c >> 1));
  const int ra1 = ra0 ^ 64;

  bf16x8 fa[2][MT], fb[2][7];
  auto sync = [&](auto S) {
    constexpr int s = S;
    if constexpr (NSLB > NSB) asm volatile("s_waitcnt vmcnt(%0)" ::"i"(SC::wait(s)) : "memory");
    else asm volatile("s_waitcnt vmcnt(%0) lgkmcnt(0)" ::"i"(SC::wait(s)) : "memory");
    __builtin_amdgcn_s_barrier();
    if constexpr (BKIND == 2 && s == 0 && GPRIO > 0) __builtin_amdgcn_s_setprio(1);
    if constexpr (BKIND == 2 && s == GPRIO && GPRIO > 0) __builtin_amdgcn_s_setprio(0);
    if constexpr (ADIR && (s & 1) == 0) static_for<MT>([&](auto M) { tie2(fd[s][M], fd[s + 1][M]); });
    __builtin_amdgcn_sched_barrier(0);
  };
  constexpr int NRA = ADIR ? 0 : MT;
  constexpr int NSIDE = NRA + 7 + 2 + MT;
  auto side = [&](auto S, auto K) {
    constexpr int s = S, k = K;
    if constexpr (k < NRA) {
      const char* sa = smem + ((s / 2) % NSA) * ASLOT + ((s & 1) ? ra1 : ra0);
      fa[s & 1][k] = *(const bf16x8*)(sa + k * 2048);
    } else if constexpr (k < NRA + 7) {
      constexpr int nt = k - NRA;
      const char* sb = smem + BOFF + (s % NSLB) * 7168 + rb;
      if constexpr (BTR) {
        s16x4 lo = __builtin_amdgcn_ds_read_tr16_b64_v4i16((LDSP s16x4*)(sb + 32 * nt));
        s16x4 hi = __builtin_amdgcn_ds_read_tr16_b64_v4i16((LDSP s16x4*)(sb + 8 * 224 + 32 * nt));
        typedef short s16x8 __attribute__((ext_vector_type(8)));
        s16x8 t = __builtin_shufflevector(lo, hi, 0, 1, 2, 3, 4, 5, 6, 7);
        fb[s & 1][nt] = __builtin_bit_cast(bf16x8, t);
      } else {
        fb[s & 1][nt] = *(const bf16x8*)(sb + nt * 1024);
      }
    } else {
      stepload(S, std::integral_constant<int, k - NRA - 7>{});
    }
    __builtin_amdgcn_sched_barrier(0);
  };
  sync(std::integral_constant<int, 0>{});
  static_for<NSIDE>([&](auto K) { side(std::integral_constant<int, 0>{}, K); });
  static_for<NS>([&](auto S) {
    constexpr int s = S;
    if constexpr (s + 1 < NS) sync(std::integral_constant<int, s + 1>{});
    static_for<7>([&](auto N) {
      constexpr int nt = N;
      static_for<MT>([&](auto M) {
        constexpr int m = M;
        const bf16x8 av = ADIR ? fd[ADIR ? s : 0][m] : fa[s & 1][m];
        if constexpr (SWAP)
          acc[m][nt] = __builtin_amdgcn_mfma_f32_16x16x32_bf16(fb[s & 1][nt], av, acc[m][nt], 0, 0, 0);
        else
          acc[m][nt] = __builtin_amdgcn_mfma_f32_16x16x32_bf16(av, fb[s & 1][nt], acc[m][nt], 0, 0, 0);
        __builtin_amdgcn_sched_barrier(0);
        if constexpr (s + 1 < NS) {
          static_for<NSIDE>([&](auto K) {
            if constexpr ((K * 7 * MT) / NSIDE == nt * MT + m) side(std::integral_constant<int, s + 1>{}, K);
          });
        }
      });
    });
  });
}

template <int MT>
__device__ __forceinline__ void a_offsets(unsigned (&avoff)[2 * MT], int m0, int lda2, int lane, int maxrow) {
#pragma unroll
  for (int p = 0; p < 2 * MT; ++p) {
    const int r = 8 * p + (lane >> 3);
    int row = m0 + r;
    row = row < maxrow ? row : maxrow;
    avoff[p] = row * lda2 + 16 * ((lane & 7) ^ ((r >> 1) & 7));
  }
}

template <int MT>
__device__ __forceinline__ void store_rows(const f32x4 (&acc)[MT][7], char* tb, int tstride, char* dst, int ld2, int lane) {
  const int c = lane & 15, g = lane >> 4;
#pragma unroll
  for (int m = 0; m < MT; ++m)
#pragma unroll
    for (int n = 0; n < 7; ++n)
      *(u32x2*)(tb + m * tstride + c * 240 + n * 32 + g * 8) = u32x2{pack2(acc[m][n][0], acc[m][n][1]), pack2(acc[m][n][2], acc[m][n][3])};
#pragma unroll
  for (int m = 0; m < MT; ++m)
#pragma unroll
    for (int i = 0; i < 4; ++i) {
      const int u = i * 64 + lane;
      const int row = (u * 4682) >> 16, ch = u - row * 14;
      if (i < 3 || lane < 32) {
        const u32x4 v = *(const u32x4*)(tb + m * tstride + row * 240 + ch * 16);
        st16<MT == 2 ? WT2 : WT3>(dst + (size_t)(m * 16 + row) * ld2 + ch * 16, v);
      }
    }
}

constexpr int K1_MT = 3;
#define K1CFG 7, 3, 4
__global__ void __launch_bounds__(256, 1)
k1_proj(const __bf16* __restrict__ xa, const __bf16* __restrict__ wqkv, const __bf16* __restrict__ xb,
        const __bf16* __restrict__ whr, const __bf16* __restrict__ whi, char* __restrict__ ws) {
  extern __shared__ __attribute__((aligned(16))) char smem[];
  const int lane = threadIdx.x & 63;
  const int wave = __builtin_amdgcn_readfirstlane(threadIdx.x >> 6);
  const int bid = blockIdx.x;
  TSDECL TS(0); TS(1);
  const int xcd = bid & 7, idx = bid >> 3;
  unsigned sink = 0;
  {
    const char* pc = code_ptr();
    const int L = (idx * 4 + wave) * 64 + lane;
    if (L < IC1) touch(sink, pc + L * 128);
  }
  const int rt = idx / 10, ci = idx - 10 * rt;
  const int ct = (xcd & 1) * 10 + ci;
  const int lim = (xcd >> 1) * 512 + 512;
  const int m0 = (xcd >> 1) * 512 + rt * 192 + wave * 48;
  const int rot = (ci + 3 * rt) % 7;
  const int c = lane & 15, g = lane >> 4;
  unsigned avoff[2 * K1_MT];
  a_offsets<K1_MT>(avoff, m0, DM * 2, lane, lim - 1);
  f32x4 acc[K1_MT][7];
#pragma unroll
  for (int m = 0; m < K1_MT; ++m)
#pragma unroll
    for (int n = 0; n < 7; ++n) acc[m][n] = f32x4{0.f, 0.f, 0.f, 0.f};

  char* tb = smem + wave * 6144;
  if (ct < 8) {
    gemm_core<K1CFG, K1_MT, 0, true>((const char*)xa, avoff, (const char*)wqkv + ct * 224, 3 * DM * 2, smem, wave, lane, rot, acc);
    TS(2);
    const bool isq = ct < 4;
    const float sc = isq ? 0.18898223650461362f * 1.4426950408889634f : 1.0f;
    char* dst = ws + (isq ? WS_Q : WS_K);
    const unsigned padlo = isq ? 0u : 0x3f803f80u, padhi = isq ? 0u : 0x00003f80u;
#pragma unroll
    for (int m = 0; m < K1_MT; ++m) {
      char* st = tb + m * 24576;
#pragma unroll
      for (int nt = 0; nt < 7; ++nt) {
        const int n4 = 4 * nt + g;
        const int hh = (n4 * 37) >> 8;
        const int q4 = n4 - 7 * hh;
        char* p = st + (hh * 4 + (q4 >> 1)) * 256 + c * 16 + (q4 & 1) * 8;
        *(u32x2*)p = u32x2{pack2(acc[m][nt][0] * sc, acc[m][nt][1] * sc), pack2(acc[m][nt][2] * sc, acc[m][nt][3] * sc)};
        if (q4 == 6) *(u32x2*)(p + 8) = u32x2{padlo, padhi};
      }
    }
#pragma unroll
    for (int m = 0; m < K1_MT; ++m) {
      const int t0 = m0 + 16 * m;
      if (t0 < lim) {
#pragma unroll
        for (int hh = 0; hh < 4; ++hh) {
          const u32x4 v = *(const u32x4*)(tb + m * 24576 + hh * 1024 + lane * 16);
          const int lo = isq ? (t0 & 16) * 64 + c * 16 : ((c >> 2) & 1) * 1024 + ((t0 & 16) / 2 + (c >> 3) * 4 + (c & 3)) * 16;
          st16<WT1>(dst + ((4 * (ct & 3) + hh) * 64 + (t0 >> 5)) * 2048 + g * 256 + lo, v);
        }
      }
    }
  } else if (ct < 12) {
    gemm_core<K1CFG, K1_MT, 0, false>((const char*)xa, avoff, (const char*)wqkv + ct * 224, 3 * DM * 2, smem, wave, lane, rot, acc);
    TS(2);
    char* dst = ws + WS_V;
#pragma unroll
    for (int m = 0; m < K1_MT; ++m) {
      char* st = tb + m * 24576;
#pragma unroll
      for (int nt = 0; nt < 7; ++nt) {
        const int nl = 16 * nt + c;
        const int hh = (nl * 147) >> 12;
        const int d = nl - 28 * hh;
        char* p = st + hh * 1024 + (d >> 4) * 512 + (g >> 1) * 256 + (d & 15) * 16 + (g & 1) * 8;
        *(u32x2*)p = u32x2{pack2(acc[m][nt][0], acc[m][nt][1]), pack2(acc[m][nt][2], acc[m][nt][3])};
      }
      if (lane < 32) {
        const unsigned o1 = (lane & 3) == 0 ? 0x3f803f80u : 0u;
        *(u32x4*)(st + (lane >> 3) * 1024 + 512 + ((lane >> 2) & 1) * 256 + (12 + (lane & 3)) * 16) = u32x4{o1, o1, o1, o1};
      }
    }
#pragma unroll
    for (int m = 0; m < K1_MT; ++m) {
      const int t0 = m0 + 16 * m;
      if (t0 < lim) {
#pragma unroll
        for (int hh = 0; hh < 4; ++hh) {
          const u32x4 v = *(const u32x4*)(tb + m * 24576 + hh * 1024 + lane * 16);
          st16<WT1>(dst + ((4 * (ct & 3) + hh) * 64 + (t0 >> 5)) * 2048 + (lane >> 5) * 1024 + (t0 & 16) * 32 + (lane & 31) * 16, v);
        }
      }
    }
  } else {
    const bool isb = ct >= 16;
    const int nq = ct & 3;
    const char* W = (const char*)(isb ? whi : whr) + (size_t)nq * 112 * (DM * 2);
    gemm_core<K1CFG, K1_MT, 1, false>((const char*)xb, avoff, W, DM * 2, smem, wave, lane, rot, acc);
    TS(2);
    const float sg = isb ? -1.f : 1.f;
    char* dst = ws + WS_B;
#pragma unroll
    for (int m = 0; m < K1_MT; ++m) {
      char* st = tb + m * 24576;
#pragma unroll
      for (int nt = 0; nt < 7; ++nt)
        *(u32x2*)(st + nt * 512 + (g >> 1) * 256 + c * 16 + (g & 1) * 8) =
            u32x2{pack2(acc[m][nt][0] * sg, acc[m][nt][1] * sg), pack2(acc[m][nt][2] * sg, acc[m][nt][3] * sg)};
    }
#pragma unroll
    for (int m = 0; m < K1_MT; ++m) {
      const int t0 = m0 + 16 * m;
      if (t0 < lim) {
        const int kk = t0 + (isb ? SEQ : 0);
        char* gp = dst + ((kk >> 5) * 4 + nq) * 7168 + (kk & 16) * 32 + (lane >> 5) * 1024 + (lane & 31) * 16;
#pragma unroll
        for (int i = 0; i < 4; ++i) {
          if (i < 3 || lane < 32) {
            const u32x4 v = *(const u32x4*)(tb + m * 24576 + i * 1024 + lane * 16);
            st16<WT1>(gp + i * 2048, v);
          }
        }
      }
    }
  }
  keep(sink);
  TSEND(ws, 0);
}

__device__ __forceinline__ float col_max(const f32x4& a, const f32x4& b) {
  float m = fmaxf(fmaxf(fmaxf(a[0], a[1]), fmaxf(a[2], a[3])), fmaxf(fmaxf(b[0], b[1]), fmaxf(b[2], b[3])));
  auto r = __builtin_amdgcn_permlane32_swap(__builtin_bit_cast(unsigned, m), __builtin_bit_cast(unsigned, m), false, false);
  m = fmaxf(__builtin_bit_cast(float, (unsigned)r[0]), __builtin_bit_cast(float, (unsigned)r[1]));
  auto t = __builtin_amdgcn_permlane16_swap(__builtin_bit_cast(unsigned, m), __builtin_bit_cast(unsigned, m), false, false);
  return fmaxf(__builtin_bit_cast(float, (unsigned)t[0]), __builtin_bit_cast(float, (unsigned)t[1]));
}
__device__ __forceinline__ unsigned pack2p(float a, float b) {
  unsigned r = pack2(a, b);
  asm volatile("" : "+v"(r));
  return r;
}
#define MFMA16(a, b, c) __builtin_amdgcn_mfma_f32_16x16x32_bf16(a, b, c, 0, 0, 0)

__device__ __forceinline__ void attn_role(const char* __restrict__ ws, char* __restrict__ wso, char* smem, int wg, int wave, int lane) {
  TSDECL TS(0);
  const int xcd = wg & 7, idx = wg >> 3;
  const int head = xcd * 2 + (idx >> 4), qb = idx & 15;
  const bf16x8* Kf = (const bf16x8*)(ws + WS_K) + (size_t)(head * 64 + wave * 16) * 128 + lane;
  const bf16x8* Vf = (const bf16x8*)(ws + WS_V) + (size_t)(head * 64 + wave * 16) * 128 + lane;
  const f32x4 zero4 = {0.f, 0.f, 0.f, 0.f};

  const char* Kb = (const char*)(ws + WS_K) + (size_t)(head * 64 + wave * 16) * 2048;
  const unsigned l16 = lane * 16;
  bf16x8 kf[3][2], vf[3][2];
  bf16x8 qf[8];
  auto loadkv = [&](auto T) {
    constexpr int t = T;
    const char* src = Kb + t * 2048;
    gload16<0>(kf[t % 3][0], l16, src);
    gload16<1024>(kf[t % 3][1], l16, src);
    gload16<0>(vf[t % 3][0], l16, src + (WS_V - WS_K));
    gload16<1024>(vf[t % 3][1], l16, src + (WS_V - WS_K));
  };
  loadkv(std::integral_constant<int, 0>{});
  {
    const char* Qb = (const char*)(ws + WS_Q) + (size_t)(head * 64 + qb * 4) * 2048;
    static_for<4>([&](auto QT) {
      gload16<0>(qf[2 * QT], l16, Qb + QT * 2048);
      gload16<1024>(qf[2 * QT + 1], l16, Qb + QT * 2048);
    });
  }
  loadkv(std::integral_constant<int, 1>{});
  asm volatile("s_waitcnt vmcnt(4)" ::: "memory");
  tie2(kf[0][0], kf[0][1]); tie2(vf[0][0], vf[0][1]);
#pragma unroll
  for (int j = 0; j < 4; ++j) tie2(qf[2 * j], qf[2 * j + 1]);
  const bool top = lane >= 48;
  float mref[8];
#pragma unroll
  for (int j = 0; j < 8; ++j) {
    const f32x4 sa = MFMA16(kf[0][0], qf[j], zero4), sb = MFMA16(kf[0][1], qf[j], zero4);
    const float nm = -(col_max(sa, sb) + 64.f);
    const __bf16 b0 = (__bf16)nm;
    mref[j] = -(float)b0;
    if (top) qf[j][4] = b0;
  }
  TS(1);
  f32x4 o[8][2];
#pragma unroll
  for (int j = 0; j < 8; ++j) { o[j][0] = zero4; o[j][1] = zero4; }

  {
#define SB() __builtin_amdgcn_sched_barrier(0)
    f32x4 s0[2], s1[2];
    u32x4 p[2];
    float ea[2], eb[2];
    s0[0] = MFMA16(kf[0][0], qf[0], zero4);
    s1[0] = MFMA16(kf[0][1], qf[0], zero4);
    SB();
    static_for<128>([&](auto I) {
      constexpr int i = I, kt = i >> 3, j = i & 7;
      constexpr int ni = i + 1, nkt = ni >> 3, nj = ni & 7;
      constexpr int pi = i - 1, pkt = pi >> 3, pj = pi & 7;
      if constexpr (j == 7 && ni < 128) {
        asm volatile("s_waitcnt vmcnt(%0)" ::"i"(kt + 2 < 16 ? 4 : 0) : "memory");
        tie2(kf[nkt % 3][0], kf[nkt % 3][1]); tie2(vf[nkt % 3][0], vf[nkt % 3][1]);
      }
      if constexpr (ni < 128) s0[ni & 1] = MFMA16(kf[nkt % 3][0], qf[nj], zero4);
      SB();
      ea[0] = __builtin_amdgcn_exp2f(s0[i & 1][0]); ea[1] = __builtin_amdgcn_exp2f(s0[i & 1][1]);
      SB();
      if constexpr (i > 0) p[pi & 1][3] = pack2p(eb[0], eb[1]);
      SB();
      if constexpr (ni < 128) s1[ni & 1] = MFMA16(kf[nkt % 3][1], qf[nj], zero4);
      SB();
      eb[0] = __builtin_amdgcn_exp2f(s0[i & 1][2]); eb[1] = __builtin_amdgcn_exp2f(s0[i & 1][3]);
      SB();
      p[i & 1][0] = pack2p(ea[0], ea[1]);
      if constexpr (j == 1 && kt + 2 < 16) loadkv(std::integral_constant<int, kt + 2>{});
      SB();
      if constexpr (i > 0) o[pj][0] = MFMA16(vf[pkt % 3][0], __builtin_bit_cast(bf16x8, p[pi & 1]), o[pj][0]);
      SB();
      ea[0] = __builtin_amdgcn_exp2f(s1[i & 1][0]); ea[1] = __builtin_amdgcn_exp2f(s1[i & 1][1]);
      SB();
      p[i & 1][1] = pack2p(eb[0], eb[1]);
      SB();
      if constexpr (i > 0) o[pj][1] = MFMA16(vf[pkt % 3][1], __builtin_bit_cast(bf16x8, p[pi & 1]), o[pj][1]);
      SB();
      eb[0] = __builtin_amdgcn_exp2f(s1[i & 1][2]); eb[1] = __builtin_amdgcn_exp2f(s1[i & 1][3]);
      SB();
      p[i & 1][2] = pack2p(ea[0], ea[1]);
      SB();
    });
    p[1][3] = pack2(eb[0], eb[1]);
    o[7][0] = MFMA16(vf[15 % 3][0], __builtin_bit_cast(bf16x8, p[1]), o[7][0]);
    o[7][1] = MFMA16(vf[15 % 3][1], __builtin_bit_cast(bf16x8, p[1]), o[7][1]);
  }

  TS(2);
  bool bad = false;
  {
    float am = 0.f;
#pragma unroll
    for (int j = 0; j < 8; ++j) {
      const float l = o[j][1][0];
      bad |= top && !(l >= 0x1p-80f && l <= 0x1p64f);
#pragma unroll
      for (int e = 0; e < 4; ++e) am += fabsf(o[j][0][e]) + fabsf(o[j][1][e]);
    }
    bad |= !(am <= 0x1p120f);
  }
  if (__any(bad)) {
#pragma unroll
    for (int j = 0; j < 8; ++j) {
      if (top) qf[j][4] = (__bf16)0.f;
      o[j][0] = zero4; o[j][1] = zero4;
      mref[j] = -INFINITY;
    }
#pragma unroll 1
    for (int kt = 0; kt < 16; ++kt) {
      const bf16x8 k0 = Kf[kt * 128], k1 = Kf[kt * 128 + 64], v0 = Vf[kt * 128], v1 = Vf[kt * 128 + 64];
#pragma unroll
      for (int j = 0; j < 8; ++j) {
        const f32x4 sa = MFMA16(k0, qf[j], zero4), sb = MFMA16(k1, qf[j], zero4);
        const float mn = fmaxf(mref[j], col_max(sa, sb));
        const float al = __builtin_amdgcn_exp2f(mref[j] - mn);
        mref[j] = mn;
        bf16x8 pp;
#pragma unroll
        for (int e = 0; e < 4; ++e) {
          pp[e] = (__bf16)__builtin_amdgcn_exp2f(sa[e] - mn);
          pp[4 + e] = (__bf16)__builtin_amdgcn_exp2f(sb[e] - mn);
          o[j][0][e] *= al; o[j][1][e] *= al;
        }
        o[j][0] = MFMA16(v0, pp, o[j][0]);
        o[j][1] = MFMA16(v1, pp, o[j][1]);
      }
    }
  }

  f32x4* part = (f32x4*)smem;
  float* mbuf = (float*)(smem + 65536);
#pragma unroll
  for (int j = 0; j < 8; ++j) {
    mbuf[(j * 4 + wave) * 64 + lane] = top ? mref[j] + __builtin_amdgcn_logf(o[j][1][0]) : mref[j];
    if ((j >> 1) != wave) {
      part[((j * 4 + wave) * 2 + 0) * 64 + lane] = o[j][0];
      part[((j * 4 + wave) * 2 + 1) * 64 + lane] = o[j][1];
    }
  }
  f32x4 mine[2][2];
#pragma unroll
  for (int jj = 0; jj < 2; ++jj)
#pragma unroll
    for (int dt = 0; dt < 2; ++dt)
      mine[jj][dt] = wave == 0 ? o[jj][dt] : (wave == 1 ? o[2 + jj][dt] : (wave == 2 ? o[4 + jj][dt] : o[6 + jj][dt]));
  __syncthreads();
  const int c = lane & 15, g = lane >> 4;
#pragma unroll
  for (int jj = 0; jj < 2; ++jj) {
    const int j = 2 * wave + jj;
    float mw[4];
#pragma unroll
    for (int w = 0; w < 4; ++w) mw[w] = mbuf[(j * 4 + w) * 64 + 48 + c];
    const float mm = fmaxf(fmaxf(mw[0], mw[1]), fmaxf(mw[2], mw[3]));
#pragma unroll
    for (int w = 0; w < 4; ++w) mw[w] = mbuf[(j * 4 + w) * 64 + c];
    f32x4 r[2] = {zero4, zero4};
#pragma unroll
    for (int w = 0; w < 4; ++w) {
      const float f = __builtin_amdgcn_exp2f(mw[w] - mm);
#pragma unroll
      for (int dt = 0; dt < 2; ++dt) {
        f32x4 t;
        if (w == wave) t = mine[jj][dt];
        else t = part[((j * 4 + w) * 2 + dt) * 64 + lane];
#pragma unroll
        for (int e = 0; e < 4; ++e) r[dt][e] += f * t[e];
      }
    }
    const float inv = 1.0f / __shfl(r[1][0], 48 + c);
    const unsigned x0 = pack2(r[0][0] * inv, r[0][1] * inv), x1 = pack2(r[0][2] * inv, r[0][3] * inv);
    unsigned y0 = pack2(r[1][0] * inv, r[1][1] * inv), y1 = pack2(r[1][2] * inv, r[1][3] * inv);
    if (top) { y0 = 0u; y1 = 0u; }
    auto sa = __builtin_amdgcn_permlane16_swap(x0, y0, false, false);
    auto sb = __builtin_amdgcn_permlane16_swap(x1, y1, false, false);
    char* op = wso + WS_O + ((size_t)(qb * 8 + j) * 16 + head) * 1024 + ((g & 1) * 2 + (g >> 1)) * 256 + c * 16;
    st16<WT2>(op, u32x4{(unsigned)sa[0], (unsigned)sb[0], (unsigned)sa[1], (unsigned)sb[1]});
  }
  TSEND(wso, 1);
}

__global__ void __launch_bounds__(256, 2)
k2_main(const __bf16* __restrict__ csr, const __bf16* __restrict__ csi, const __bf16* __restrict__ wo, char* __restrict__ ws,
        __bf16* __restrict__ out) {
  extern __shared__ __attribute__((aligned(16))) char smem[];
  const int lane = threadIdx.x & 63;
  const int wave = __builtin_amdgcn_readfirstlane(threadIdx.x >> 6);
  const int bid = blockIdx.x;
  const char* pc = code_ptr();
  if (bid < 256) {
    attn_role(ws, ws, smem, bid, wave, lane);
  } else {
    TSDECL TS(0); TS(1);
    const int wg = bid - 256;
    const int xcd = wg & 7, idx = wg >> 3;
    const int ks = xcd >> 1, mt = (xcd & 1) * 8 + (idx >> 2), nq = idx & 3;
    const int m0 = mt * 128 + wave * 32;
    unsigned sink0 = 0;
    {
      const int L = (idx * 4 + wave) * 64 + lane;
      if (L < IC2) touch(sink0, pc + L * 128);
    }
    unsigned avoff[4];
    a_offsets<2>(avoff, m0, SEQ * 2, lane, SEQ - 1);
    const char* A = (const char*)(ks < 2 ? csr : csi) + (ks & 1) * 2048;
    const char* B = ws + WS_B + ((size_t)(ks * 32) * 4 + nq) * 7168;
    f32x4 acc[2][7];
#pragma unroll
    for (int m = 0; m < 2; ++m)
#pragma unroll
      for (int n = 0; n < 7; ++n) acc[m][n] = f32x4{0.f, 0.f, 0.f, 0.f};
    gemm_core<K2CFG, 2, 2, true>(A, avoff, B, 4 * 7168, smem, wave, lane, K2ROT, acc);
    TS(2);
    unsigned sink = 0;
    if (lane < 25) {
      const int L = (idx * 4 + wave) * 25 + lane;
      touch(sink, (const char*)wo + (L < 3136 ? L : 3135) * 128);
    }
    char* pd = ks == 0 ? (char*)out + (size_t)m0 * (4 * DM) + DM * 2 : ws + WS_P + ((size_t)ks * SEQ + m0) * (DM * 2);
    store_rows<2>(acc, smem + wave * 4096, 16384, pd + nq * 224, ks == 0 ? 4 * DM : 2 * DM, lane);
    keep(sink); keep(sink0);
    TSEND(ws, 1);
  }
}

__global__ void __launch_bounds__(256, 1)
k3_final(const __bf16* __restrict__ wo, const char* __restrict__ ws, __bf16* __restrict__ out) {
  extern __shared__ __attribute__((aligned(16))) char smem[];
  const int lane = threadIdx.x & 63;
  const int wave = __builtin_amdgcn_readfirstlane(threadIdx.x >> 6);
  const int bid = blockIdx.x;
  TSDECL TS(0); TS(1); TS(2);
  const int xcd = bid & 7, idx = bid >> 3;
  const char* pc = code_ptr();
  if (idx < 16) {
    const int rt = xcd * 4 + (idx >> 2), nq = idx & 3;
    const int m0 = rt * 64 + wave * 16;
    const unsigned avoff[2] = {(unsigned)(lane * 16), 0u};
    f32x4 acc[1][7];
#pragma unroll
    for (int n = 0; n < 7; ++n) acc[0][n] = f32x4{0.f, 0.f, 0.f, 0.f};
    gemm_core<K3CFG, 1, 3, true>(ws + WS_O + (size_t)(m0 >> 4) * 16384, avoff, (const char*)wo + nq * 224, DM * 2, smem, wave, lane,
                                    K3ROT, acc);
    store_rows<1>(acc, smem + 16 * 7168 + wave * 4096, 0, (char*)out + (size_t)m0 * (2 * DM * 2) + nq * 224, 2 * DM * 2, lane);
  } else {
    const int u0 = (xcd * 14 + idx - 16) * 1024 + threadIdx.x;
    u32x4 t[4][4];
    unsigned sink = 0;
    if (idx == 16 && threadIdx.x < IC3) touch(sink, pc + threadIdx.x * 128);
    char* op[4];
#pragma unroll
    for (int n = 0; n < 4; ++n) {
      const int u = u0 + n * 256, row = u / 56, ch = u - row * 56;
      op[n] = (char*)out + (size_t)row * (2 * DM * 2) + DM * 2 + ch * 16;
      t[n][0] = *(const u32x4*)op[n];
#pragma unroll
      for (int ks = 1; ks < 4; ++ks) t[n][ks] = *(const u32x4*)(ws + WS_P + (size_t)ks * (SEQ * DM * 2) + (size_t)u * 16);
    }
#pragma unroll
    for (int n = 0; n < 4; ++n) {
      u32x4 r;
#pragma unroll
      for (int e = 0; e < 4; ++e) {
        float lo = 0.f, hi = 0.f;
#pragma unroll
        for (int ks = 0; ks < 4; ++ks) {
          lo += __builtin_bit_cast(float, t[n][ks][e] << 16);
          hi += __builtin_bit_cast(float, t[n][ks][e] & 0xffff0000u);
        }
        r[e] = pack2(lo, hi);
      }
      st16<WT3>(op[n], r);
    }
    keep(sink);
  }
  TSEND(ws, 2);
}

extern "C" void kernel_launch(void* const* d_in, const int* in_sizes, int n_in, void* d_out, int out_size,
                              void* d_ws, size_t ws_size, hipStream_t stream) {
  const __bf16* xa = (const __bf16*)d_in[0];
  const __bf16* wqkv = (const __bf16*)d_in[1];
  const __bf16* wo = (const __bf16*)d_in[2];
  const __bf16* xb = (const __bf16*)d_in[3];
  const __bf16* csr = (const __bf16*)d_in[4];
  const __bf16* csi = (const __bf16*)d_in[5];
  const __bf16* whr = (const __bf16*)d_in[6];
  const __bf16* whi = (const __bf16*)d_in[7];
  char* ws = (char*)d_ws;
  constexpr int L1 = 3 * 24576 + 5 * 7168, L2 = 81920, L3 = 16 * 7168 + 4 * 4096;
  (void)hipFuncSetAttribute((const void*)k1_proj, hipFuncAttributeMaxDynamicSharedMemorySize, L1);
  (void)hipFuncSetAttribute((const void*)k2_main, hipFuncAttributeMaxDynamicSharedMemorySize, L2);
  (void)hipFuncSetAttribute((const void*)k3_final, hipFuncAttributeMaxDynamicSharedMemorySize, L3);
  k1_proj<<<240, 256, L1, stream>>>(xa, wqkv, xb, whr, whi, ws);
  k2_main<<<512, 256, L2, stream>>>(csr, csi, wo, ws, (__bf16*)d_out);
  k3_final<<<240, 256, L3, stream>>>(wo, ws, (__bf16*)d_out);
}
```
